```python
import torch

def _softmax(x, axis=-1):
    return torch.softmax(x, dim=axis)
def _gelu(x):
    return torch.nn.functional.gelu(x, approximate="tanh")
def _silu(x):
    return torch.nn.functional.silu(x)
def _sigmoid(x):
    return torch.sigmoid(x)
def _layernorm(x, axis=-1, eps=1e-5):
    m = x.mean(dim=axis, keepdim=True)
    v = x.var(dim=axis, keepdim=True, unbiased=False)
    return (x - m) / torch.sqrt(v + eps)
def _scatter_add(tgt, idx, src, axis=0):
    return tgt.clone().index_add_(axis, idx, src)
def _segment_sum(src, seg, n):
    out = torch.zeros((n,) + src.shape[1:], dtype=src.dtype)
    return out.index_add_(0, seg, src)
def _pad(x, pw):
    flat = tuple(v for pair in reversed(pw) for v in pair)
    return torch.nn.functional.pad(x, flat)

kernel_name = 'gated_mix__ssm_conv_s5__swiglu_ffn__prompt'

def setup_inputs():

    _w = (lambda a: (lambda t: t.to(torch.bfloat16) if t.is_floating_point() else t)(torch.as_tensor(a)))
    def _A_setup():
        B, S, D = 1, 2048, 512
        P = D
        Lambda_re = -0.5 * (lambda _x: torch.exp(torch.as_tensor(_x)))((lambda *s: torch.randn(*s, dtype=torch.bfloat16) * 0.2)(P))
        Lambda_im = (lambda *s: torch.randn(*s, dtype=torch.bfloat16) * 0.4)(P) * 1.0
        B_re = (lambda *s: torch.randn(*s, dtype=torch.bfloat16) * 0.2)(P, D) * (D ** -0.5)
        B_im = (lambda *s: torch.randn(*s, dtype=torch.bfloat16) * 0.2)(P, D) * (D ** -0.5)
        C_re = (lambda *s: torch.randn(*s, dtype=torch.bfloat16) * 0.2)(D, P) * (P ** -0.5)
        C_im = (lambda *s: torch.randn(*s, dtype=torch.bfloat16) * 0.2)(D, P) * (P ** -0.5)
        log_dt = (lambda *s: torch.randn(*s, dtype=torch.bfloat16) * 0.2)(P) * 0.3 - 2.0
        Dskip = (lambda *s: torch.randn(*s, dtype=torch.bfloat16) * 0.05)(D)
        return {
            "x": _w((lambda *s: torch.randn(*s, dtype=torch.bfloat16) * 0.05)(B, S, D)),
            "Lambda_re": _w(Lambda_re),
            "Lambda_im": _w(Lambda_im),
            "B_re": _w(B_re),
            "B_im": _w(B_im),
            "C_re": _w(C_re),
            "C_im": _w(C_im),
            "log_dt": _w(log_dt),
            "Dskip": _w(Dskip),
        }
    _A_in = _A_setup()

    def _B_setup():
        B, S, D, F = 1, 2048, 512, 1792
        return {
            "x": _w((lambda *s: torch.randn(*s, dtype=torch.bfloat16) * 0.05)(B, S, D)),
            "w_gate": _w((lambda *s: torch.randn(*s, dtype=torch.bfloat16) * 0.05)(D, F)),
            "w_up": _w((lambda *s: torch.randn(*s, dtype=torch.bfloat16) * 0.05)(D, F)),
            "w_down": _w((lambda *s: torch.randn(*s, dtype=torch.bfloat16) * 0.05)(F, D)),
        }
    _B_in = _B_setup()

    return {
        "A_x": _A_in["x"],
        "A_Lambda_re": _A_in["Lambda_re"],
        "A_Lambda_im": _A_in["Lambda_im"],
        "A_B_re": _A_in["B_re"],
        "A_B_im": _A_in["B_im"],
        "A_C_re": _A_in["C_re"],
        "A_C_im": _A_in["C_im"],
        "A_log_dt": _A_in["log_dt"],
        "A_Dskip": _A_in["Dskip"],
        "B_x": _B_in["x"],
        "B_w_gate": _B_in["w_gate"],
        "B_w_up": _B_in["w_up"],
        "B_w_down": _B_in["w_down"],
        "gate_w": _w((lambda *s: torch.randn(*s, dtype=torch.bfloat16) * 0.05)(512) * 16.0),
    }

def reference(A_x, A_Lambda_re, A_Lambda_im, A_B_re, A_B_im, A_C_re, A_C_im, A_log_dt, A_Dskip, B_x, B_w_gate, B_w_up, B_w_down, gate_w):
    def _A_ref(x, Lambda_re, Lambda_im, B_re, B_im, C_re, C_im, log_dt, Dskip):
        Bsz, L, H = x.shape
        Lam = Lambda_re + 1j * Lambda_im
        Bmat = B_re + 1j * B_im
        Cmat = C_re + 1j * C_im
        dt = torch.exp(log_dt)
        Lam_bar = torch.exp(dt * Lam)
        B_bar = ((Lam_bar - 1.0) / Lam)[:, None] * Bmat
        Bu = torch.einsum("bld,pd->blp", x + 0j, B_bar)
        h = torch.zeros_like(Bu[:, 0, :])
        hs = []
        for t in range(L):
            h = Lam_bar[None, :] * h + Bu[:, t, :]
            hs.append(h)
        hseq = torch.stack(hs, 1)
        y = 2.0 * torch.einsum("blp,dp->bld", hseq, Cmat).real + x * Dskip[None, None, :]
        return y

    def _B_ref(x, w_gate, w_up, w_down):
        g = _silu(x @ w_gate)
        u = x @ w_up
        return (g * u) @ w_down

    x = A_x
    g = _sigmoid((x * gate_w).sum(-1, keepdims=True))
    a = _A_ref(x, A_Lambda_re, A_Lambda_im, A_B_re, A_B_im, A_C_re, A_C_im, A_log_dt, A_Dskip)
    b = _B_ref(x, B_w_gate, B_w_up, B_w_down)
    return g * a + (1 - g) * b
```

```cpp
#include <hip/hip_runtime.h>
#include <hip/hip_bf16.h>
typedef __hip_bfloat16 bf16;
#define S 2048
#define D 512
#define P 512
#define F 1792
__device__ __forceinline__ float b2f(bf16 v){ return __bfloat162float(v);}
__device__ __forceinline__ float rbf(float v){ return __bfloat162float(__float2bfloat16(v)); }

__global__ void k_bu(const bf16* x, const bf16* Bre, const bf16* Bim, const bf16* Lre, const bf16* Lim, const bf16* ldt, float* bure, float* buim){
  int t = blockIdx.x; int p = threadIdx.x;
  float sr=0, si=0;
  for(int d=0; d<D; ++d){ float xv=b2f(x[t*D+d]); sr += xv*b2f(Bre[p*D+d]); si += xv*b2f(Bim[p*D+d]); }
  float dt = rbf(expf(b2f(ldt[p])));
  float lr=b2f(Lre[p]), li=b2f(Lim[p]);
  float e = expf(dt*lr); float ar = e*cosf(dt*li), ai = e*sinf(dt*li);
  float nr = ar-1.f, ni = ai; float den = lr*lr+li*li;
  float cr = (nr*lr+ni*li)/den, ci = (ni*lr-nr*li)/den;
  bure[t*P+p] = cr*sr-ci*si; buim[t*P+p] = cr*si+ci*sr;
}
__global__ void k_scan(const bf16* Lre, const bf16* Lim, const bf16* ldt, float* bure, float* buim){
  int p = blockIdx.x*64+threadIdx.x;
  float dt = rbf(expf(b2f(ldt[p])));
  float lr=b2f(Lre[p]), li=b2f(Lim[p]);
  float e = expf(dt*lr); float ar = e*cosf(dt*li), ai = e*sinf(dt*li);
  float hr=0, hi=0;
  for(int t=0;t<S;++t){ float nr = ar*hr-ai*hi+bure[t*P+p]; float ni = ar*hi+ai*hr+buim[t*P+p]; hr=nr; hi=ni; bure[t*P+p]=hr; buim[t*P+p]=hi; }
}
__global__ void k_act(const bf16* x, const bf16* wg, const bf16* wu, float* act){
  int t = blockIdx.x;
  for(int f=threadIdx.x; f<F; f+=blockDim.x){
    float g=0,u=0;
    for(int d=0; d<D; ++d){ float xv=b2f(x[t*D+d]); g += xv*b2f(wg[d*F+f]); u += xv*b2f(wu[d*F+f]); }
    g = rbf(g); u = rbf(u);
    float s = rbf(g/(1.f+expf(-g)));
    act[t*F+f] = rbf(s*u);
  }
}
__global__ void k_out(const bf16* x, const float* hre, const float* him, const bf16* Cre, const bf16* Cim, const bf16* Dsk, const float* act, const bf16* wd, const bf16* gw, float* out){
  int t = blockIdx.x; int d = threadIdx.x;
  float gs=0; for(int k=0;k<D;++k) gs += rbf(b2f(x[t*D+k])*b2f(gw[k]));
  gs = rbf(gs); float g = rbf(1.f/(1.f+expf(-gs)));
  float y=0; for(int p=0;p<P;++p) y += hre[t*P+p]*b2f(Cre[d*P+p]) - him[t*P+p]*b2f(Cim[d*P+p]);
  float a = 2.f*y + rbf(b2f(x[t*D+d])*b2f(Dsk[d]));
  float b=0; for(int f=0;f<F;++f) b += act[t*F+f]*b2f(wd[f*D+d]);
  b = rbf(b);
  out[t*D+d] = g*a + rbf(rbf(1.f-g)*b);
}
extern "C" void kernel_launch(void* const* d_in, const int* in_sizes, int n_in, void* d_out, int out_size, void* d_ws, size_t ws_size, hipStream_t stream) {
  const bf16* x=(const bf16*)d_in[0]; const bf16* Lre=(const bf16*)d_in[1]; const bf16* Lim=(const bf16*)d_in[2];
  const bf16* Bre=(const bf16*)d_in[3]; const bf16* Bim=(const bf16*)d_in[4]; const bf16* Cre=(const bf16*)d_in[5]; const bf16* Cim=(const bf16*)d_in[6];
  const bf16* ldt=(const bf16*)d_in[7]; const bf16* Dsk=(const bf16*)d_in[8];
  const bf16* wg=(const bf16*)d_in[10]; const bf16* wu=(const bf16*)d_in[11]; const bf16* wd=(const bf16*)d_in[12]; const bf16* gw=(const bf16*)d_in[13];
  float* bure=(float*)d_ws; float* buim=bure+S*P; float* act=buim+S*P;
  k_bu<<<S,P,0,stream>>>(x,Bre,Bim,Lre,Lim,ldt,bure,buim);
  k_scan<<<P/64,64,0,stream>>>(Lre,Lim,ldt,bure,buim);
  k_act<<<S,256,0,stream>>>(x,wg,wu,act);
  k_out<<<S,D,0,stream>>>(x,bure,buim,Cre,Cim,Dsk,act,wd,gw,(float*)d_out);
}
```

```cpp
#include <hip/hip_runtime.h>
#include <hip/hip_bf16.h>

#define GAS __attribute__((address_space(1)))
#define LAS __attribute__((address_space(3)))
#define DI __device__ __forceinline__
typedef unsigned short u16;
typedef unsigned v4u __attribute__((ext_vector_type(4)));
typedef unsigned v2u __attribute__((ext_vector_type(2)));
typedef float v4f __attribute__((ext_vector_type(4)));
typedef float v2f __attribute__((ext_vector_type(2)));
typedef short s16x4 __attribute__((ext_vector_type(4)));
typedef short s16x8 __attribute__((ext_vector_type(8)));
typedef __bf16 bf16x8 __attribute__((ext_vector_type(8)));
typedef __bf16 bf16x2 __attribute__((ext_vector_type(2)));
typedef float f32x16 __attribute__((ext_vector_type(16)));

#define ABL 0
#define ROT1 0
#define ROT2 1

constexpr unsigned WS_CTL  = 0;
constexpr unsigned WS_FLAG = 4096;
constexpr unsigned WS_GRAN = WS_FLAG + 32768;
constexpr unsigned WS_G    = WS_GRAN + 131072;
constexpr unsigned WS_STAMP= 512u << 10;
constexpr unsigned WS_AH   = 1u << 20;
constexpr unsigned WS_END  = WS_AH + 64u * 176u * 1024u;

constexpr unsigned SLOT1 = 53248;
constexpr unsigned RB2   = 13, RA2 = 6;
constexpr unsigned A2OFF = RB2 * 8192;
constexpr unsigned L_EX   = A2OFF + RA2 * 8192;
constexpr unsigned MISC  = 3 * SLOT1;
constexpr unsigned L_FULL = MISC + 0;
constexpr unsigned L_PROG = MISC + 32;
constexpr unsigned L_RDY1 = MISC + 64;
constexpr unsigned L_RDY2 = MISC + 68;
constexpr unsigned L_INIT = MISC + 72;
constexpr unsigned L_CNT1 = MISC + 76;
constexpr unsigned L_CNT2 = MISC + 80;
constexpr unsigned L_EF   = MISC + 96;
constexpr unsigned L_CF   = MISC + 128;
constexpr unsigned L_TAG  = MISC + 256;
constexpr unsigned L_LAM  = MISC + 272;
constexpr unsigned L_CIN  = MISC + 2576;
constexpr unsigned LDS_BYTES = 163840;

constexpr unsigned SPIN_CAP = 1u << 20;

DI float bf2f(u16 v) { return __uint_as_float((unsigned)v << 16); }
DI unsigned pk_bf16(float a, float b) { v2f p = {a, b}; return __builtin_bit_cast(unsigned, __builtin_convertvector(p, bf16x2)); }
DI unsigned rfl(unsigned v) { return __builtin_amdgcn_readfirstlane(v); }

DI void lds_st(unsigned addr, unsigned v) { asm volatile("ds_write_b32 %0, %1" ::"v"(addr), "v"(v) : "memory"); }
DI v4u lds_ld4(unsigned addr) { v4u r; asm volatile("ds_read_b128 %0, %1\n\ts_waitcnt lgkmcnt(0)" : "=&v"(r) : "v"(addr) : "memory"); return r; }
DI unsigned lds_ld1(unsigned addr) { unsigned r; asm volatile("ds_read_b32 %0, %1\n\ts_waitcnt lgkmcnt(0)" : "=&v"(r) : "v"(addr) : "memory"); return r; }
DI unsigned lds_add_rtn(unsigned addr, unsigned v) { unsigned r; asm volatile("ds_add_rtn_u32 %0, %1, %2\n\ts_waitcnt lgkmcnt(0)" : "=&v"(r) : "v"(addr), "v"(v) : "memory"); return r; }
DI unsigned min4(v4u v) { unsigned a = v.x < v.y ? v.x : v.y, b = v.z < v.w ? v.z : v.w; return a < b ? a : b; }

DI void lds_add(unsigned addr, unsigned v) { asm volatile("ds_add_u32 %0, %1" ::"v"(addr), "v"(v) : "memory"); }
#define WAIT_VM(n) asm volatile("s_waitcnt vmcnt(%0)" ::"i"(n) : "memory")

struct C2 { float r, i; };
DI C2 cmul(C2 a, C2 b) { return {a.r * b.r - a.i * b.i, a.r * b.i + a.i * b.r}; }
DI C2 cfma(C2 a, C2 b, C2 c) { return {fmaf(a.r, b.r, fmaf(-a.i, b.i, c.r)), fmaf(a.r, b.i, fmaf(a.i, b.r, c.i))}; }
DI C2 csq(C2 a) { return {a.r * a.r - a.i * a.i, 2.f * a.r * a.i}; }
DI void swap32(float& x, float& y) { auto r = __builtin_amdgcn_permlane32_swap(__float_as_uint(x), __float_as_uint(y), false, false); x = __uint_as_float(r[0]); y = __uint_as_float(r[1]); }
DI void swap16(float& x, float& y) { auto r = __builtin_amdgcn_permlane16_swap(__float_as_uint(x), __float_as_uint(y), false, false); x = __uint_as_float(r[0]); y = __uint_as_float(r[1]); }
DI void swap32u(unsigned& x, unsigned& y) { auto r = __builtin_amdgcn_permlane32_swap(x, y, false, false); x = r[0]; y = r[1]; }

#define MFMA32(a, b, c) __builtin_amdgcn_mfma_f32_32x32x16_bf16((a), (b), (c), 0, 0, 0)
#define GLDS(g, l, aux) __builtin_amdgcn_global_load_lds((const GAS void*)(g), (LAS void*)(l), 16, 0, (aux))

DI bf16x8 ld_b128(LAS char* L, unsigned off) { return *(const LAS bf16x8*)(L + off); }
DI bf16x8 ld_tr2(LAS char* L, unsigned off, unsigned d) {
  s16x4 a = __builtin_amdgcn_ds_read_tr16_b64_v4i16((LAS s16x4*)(L + off));
  s16x4 b = __builtin_amdgcn_ds_read_tr16_b64_v4i16((LAS s16x4*)(L + off + d));
  return __builtin_bit_cast(bf16x8, __builtin_shufflevector(a, b, 0, 1, 2, 3, 4, 5, 6, 7));
}

#define ST(i) do {} while (0)
#define TS(v) do {} while (0)
#define TSET(v) do {} while (0)
#define TW(i, v) do {} while (0)

struct Wait {
  unsigned budget;
  DI bool spin() { __builtin_amdgcn_s_sleep(1); if (budget == 0) return false; --budget; return true; }
  DI unsigned min4w(unsigned addr, unsigned need) {
    unsigned v;
#pragma nounroll
    do { v = rfl(min4(lds_ld4(addr))); } while (v < need && spin());
    return v;
  }
  DI unsigned min8w(unsigned addr, unsigned need) {
    unsigned v;
#pragma nounroll
    do { unsigned a = min4(lds_ld4(addr)), b = min4(lds_ld4(addr + 16)); v = rfl(a < b ? a : b); } while (v < need && spin());
    return v;
  }
  DI void word(unsigned addr, unsigned need) {
#pragma nounroll
    while (rfl(lds_ld1(addr)) < need && spin()) {}
  }
  DI void four(unsigned addr, unsigned n) {
    unsigned ok;
#pragma nounroll
    do { v4u v = lds_ld4(addr); ok = rfl((n < 1 || v.x) && (n < 2 || v.y) && (n < 3 || v.z) && (n < 4 || v.w)); } while (!ok && spin());
  }
};

template <int NF, int FT0, int NS, int ST0, bool SSMBLK>
DI void phase1(LAS char* L, GAS char* ws, __amdgpu_buffer_rsrc_t wsr, Wait& W, int wave, int lane, int m1, int j1, unsigned& tag) {
  const int tg = wave & 3, grp = wave >> 2;
  const int h = lane >> 5, r32 = lane & 31;
  f32x16 accF[NF > 0 ? NF : 1], accS[NS > 0 ? NS : 1];
#pragma unroll
  for (int i = 0; i < NF; ++i) accF[i] = (f32x16){};
#pragma unroll
  for (int i = 0; i < NS; ++i) accS[i] = (f32x16){};
  unsigned ro[4];
#pragma unroll
  for (int ks = 0; ks < 4; ++ks) ro[ks] = r32 * 128 + (((2 * ks + h) ^ ((r32 >> 1) & 7)) << 4);
  unsigned to[NF > 0 ? NF : 1];
  {
    const int g = lane >> 4, img = g & 1, hh = g >> 1, q = (lane & 15) >> 2, p = lane & 3;
#pragma unroll
    for (int i = 0; i < NF; ++i) to[i] = 16384 + img * 16384 + (8 * hh + q) * 256 + (((FT0 + i) ^ ((q << 1) | img)) << 5) + p * 8;
  }
  constexpr unsigned SW0 = SSMBLK ? 16384u + ST0 * 4096u : 49152u;
  unsigned sb = SLOT1;
  TS(tw0); TS(tw1); TS(tw2); TS(tw3); TS(tw4); TS(tw5);
#pragma unroll 1
  for (int t = 0; t < 8; ++t) {
    W.min4w(L_FULL, t + 1);
    const unsigned xb = sb + tg * 4096;
#pragma unroll
    for (int ks = 0; ks < 4; ++ks) {
      bf16x8 xf = ld_b128(L, xb + ro[ks]);
      bf16x8 wf[NF > 0 ? NF : 1], sf[NS > 0 ? NS : 1];
#pragma unroll
      for (int i = 0; i < NF; ++i) wf[i] = ld_tr2(L, sb + to[i] + ks * 4096, 1024);
#pragma unroll
      for (int i = 0; i < NS; ++i) sf[i] = ld_b128(L, sb + ro[ks] + SW0 + i * 4096);
      if (ks == 3) {
        asm volatile("s_waitcnt lgkmcnt(0)" ::: "memory");
        lds_st(L_PROG + wave * 4, (unsigned)(t + 1));
      }
#pragma unroll
      for (int i = 0; i < NF; ++i) accF[i] = MFMA32(wf[i], xf, accF[i]);
#pragma unroll
      for (int i = 0; i < NS; ++i) accS[i] = MFMA32(xf, sf[i], accS[i]);
    }
    sb += SLOT1; if (sb == 3 * SLOT1) sb = 0;
  }
  if (wave == 0) ST(2);
  if (tg == 0) { TW(16 + grp * 6, tw0); TW(17 + grp * 6, tw1); TW(18 + grp * 6, tw2); TW(19 + grp * 6, tw3); TW(20 + grp * 6, tw4); TW(21 + grp * 6, tw5); }
  W.word(L_INIT, 8);
  tag = rfl(lds_ld1(L_TAG));
  const int c = lane & 15, rho = (lane >> 4) & 1;
  const unsigned tgG = m1 * 4 + tg;
  const int sig0 = SSMBLK ? (14 + (j1 - 14) * 9) : j1;
  if constexpr (NS > 0) {
    W.min8w(L_PROG, 8);
    C2 Eown[NS];
#pragma unroll
    for (int i = 0; i < NS; ++i) {
      f32x16& a = accS[i];
#pragma unroll
      for (int r = 0; r < 8; ++r) { float x = a[r], y = a[r + 8]; swap16(x, y); a[r] = x; a[r + 8] = y; }
      const v4f lc = *(const LAS v4f*)(L + L_LAM + ((ST0 + i) * 16 + c) * 16);
      const C2 l1 = {lc.x, lc.y}, l2 = csq(l1), l4 = csq(l2), l8 = csq(l4), l16 = csq(l8);
      C2 eE[2], eO[2], U[2];
#pragma unroll
      for (int sg = 0; sg < 2; ++sg) {
        C2 e = {a[4 * sg], a[4 * sg + 8]};
#pragma unroll
        for (int k = 1; k < 4; ++k) e = cfma(l1, e, (C2){a[4 * sg + k], a[4 * sg + k + 8]});
        eE[sg] = e; eO[sg] = e;
        swap32(eE[sg].r, eO[sg].r); swap32(eE[sg].i, eO[sg].i);
        U[sg] = cfma(l4, eE[sg], eO[sg]);
      }
      C2 V0 = cfma(l8, U[0], U[1]), V1 = V0;
      swap16(V0.r, V1.r); swap16(V0.i, V1.i);
      Eown[i] = cfma(l16, V0, V1);
      C2 c8[2];
      c8[0] = rho ? V0 : (C2){0.f, 0.f};
      c8[1] = cfma(l8, c8[0], U[0]);
#pragma unroll
      for (int sg = 0; sg < 2; ++sg) {
        C2 t4 = cfma(l4, c8[sg], eE[sg]);
        C2 c4 = h ? t4 : c8[sg];
        C2 u0 = cfma(l1, c4, (C2){a[4 * sg], a[4 * sg + 8]});
        a[4 * sg] = u0.r; a[4 * sg + 8] = u0.i;
      }
      if (tg < 3 && lane < 16) *(LAS v2f*)(L + L_EX + (((ST0 + i) * 3 + tg) * 16 + c) * 8) = (v2f){Eown[i].r, Eown[i].i};
      asm volatile("" : "+v"(a), "+v"(Eown[i].r), "+v"(Eown[i].i));
    }
    if (tg < 3) {
      asm volatile("s_waitcnt lgkmcnt(0)" ::: "memory");
      lds_st(L_EF + (grp * 4 + tg) * 4, 1u);
    } else if (m1 < 15) {
      W.four(L_EF + grp * 16, 3);
#pragma unroll
      for (int i = 0; i < NS; ++i) {
        const v4f lc = *(const LAS v4f*)(L + L_LAM + ((ST0 + i) * 16 + c) * 16);
        const C2 l32 = csq(csq(csq(csq(csq((C2){lc.x, lc.y})))));
        const v2f e0 = *(const LAS v2f*)(L + L_EX + (((ST0 + i) * 3 + 0) * 16 + c) * 8);
        const v2f e1 = *(const LAS v2f*)(L + L_EX + (((ST0 + i) * 3 + 1) * 16 + c) * 8);
        const v2f e2 = *(const LAS v2f*)(L + L_EX + (((ST0 + i) * 3 + 2) * 16 + c) * 8);
        C2 tot = cfma(l32, (C2){e0.x, e0.y}, (C2){e1.x, e1.y});
        tot = cfma(l32, tot, (C2){e2.x, e2.y});
        tot = cfma(l32, tot, Eown[i]);
        if (lane < 16) {
          GAS unsigned long long* gp = (GAS unsigned long long*)(ws + WS_GRAN + ((m1 * 32 + sig0 + ST0 + i) * 16 + c) * 16);
          __hip_atomic_store(gp, ((unsigned long long)tag << 32) | __float_as_uint(tot.r), __ATOMIC_RELAXED, __HIP_MEMORY_SCOPE_AGENT);
          __hip_atomic_store(gp + 1, ((unsigned long long)tag << 32) | __float_as_uint(tot.i), __ATOMIC_RELAXED, __HIP_MEMORY_SCOPE_AGENT);
        }
      }
    }
  }
  if constexpr (NF > 0) {
#pragma unroll
    for (int i = 0; i < NF; ++i) {
      float o[8];
#pragma unroll
      for (int r = 0; r < 8; ++r) {
        const float g = accF[i][r], u = accF[i][r + 8];
        const float e = __builtin_amdgcn_exp2f(g * -1.44269504088896f);
        o[r] = g * __builtin_amdgcn_rcpf(1.f + e) * u;
      }
      v4u v = {pk_bf16(o[0], o[1]), pk_bf16(o[2], o[3]), pk_bf16(o[4], o[5]), pk_bf16(o[6], o[7])};
      __builtin_amdgcn_raw_buffer_store_b128(v, wsr, WS_AH + (tgG * 176 + j1 * 8 + FT0 + i) * 1024 + lane * 16, 0, 16);
    }
    asm volatile("s_waitcnt vmcnt(0)" ::: "memory");
    unsigned old = 0;
    if (lane == 0) old = lds_add_rtn(L_CNT1, 1u);
    if (rfl(old) == 7u && lane == 0) __hip_atomic_store((GAS unsigned*)(ws + WS_FLAG + (m1 * 16 + j1) * 128), tag, __ATOMIC_RELAXED, __HIP_MEMORY_SCOPE_AGENT);
    if (wave == 0) ST(3);
  }
  if constexpr (NS > 0) {
    const int lg = lane >> 4;
#pragma unroll
    for (int i = 0; i < NS; ++i) {
      if ((i & 3) == tg) {
        C2 cin = {0.f, 0.f};
        if (m1 > 0) {
          const v4f lc = *(const LAS v4f*)(L + L_LAM + ((ST0 + i) * 16 + c) * 16);
          const C2 l128 = csq(csq(csq(csq(csq(csq(csq((C2){lc.x, lc.y}))))))), l256 = csq(l128), l512 = csq(l256), l384 = cmul(l128, l256);
          v4u gv[4];
#pragma nounroll
          for (;;) {
            asm volatile("" ::: "memory");
            bool ok = true;
#pragma unroll
            for (int k = 0; k < 4; ++k) {
              int mp = m1 - 1 - (4 * k + lg);
              int mc = mp < 0 ? 0 : mp;
              gv[k] = __builtin_amdgcn_raw_buffer_load_b128(wsr, WS_GRAN + ((mc * 32 + sig0 + ST0 + i) * 16 + c) * 16, 0, 16);
            }
#pragma unroll
            for (int k = 0; k < 4; ++k) ok = ok && gv[k].y == tag && gv[k].w == tag;
            if (__all(ok) || !W.spin()) break;
          }
          C2 acc = {0.f, 0.f};
#pragma unroll
          for (int k = 3; k >= 0; --k) {
            const bool valid = (m1 - 1 - (4 * k + lg)) >= 0;
            C2 e = {valid ? __uint_as_float(gv[k].x) : 0.f, valid ? __uint_as_float(gv[k].z) : 0.f};
            acc = cfma(l512, acc, e);
          }
          C2 wgt = lg == 0 ? (C2){1.f, 0.f} : lg == 1 ? l128 : lg == 2 ? l256 : l384;
          acc = cmul(acc, wgt);
          C2 o = acc; swap16(acc.r, o.r); swap16(acc.i, o.i); acc.r += o.r; acc.i += o.i;
          o = acc; swap32(acc.r, o.r); swap32(acc.i, o.i); acc.r += o.r; acc.i += o.i;
          cin = acc;
        }
        if (lane < 16) *(LAS v2f*)(L + L_CIN + ((ST0 + i) * 16 + c) * 8) = (v2f){cin.r, cin.i};
      }
    }
    asm volatile("s_waitcnt lgkmcnt(0)" ::: "memory");
    lds_st(L_CF + (grp * 4 + tg) * 4, 1u);
    W.four(L_CF + grp * 16, NS < 4 ? NS : 4);
    W.four(L_EF + grp * 16, tg);
#pragma unroll
    for (int i = 0; i < NS; ++i) {
      f32x16& a = accS[i];
      const v4f lc = *(const LAS v4f*)(L + L_LAM + ((ST0 + i) * 16 + c) * 16);
      const C2 l1 = {lc.x, lc.y}, l2 = csq(l1), l4 = csq(l2), l8 = csq(l4), l16 = csq(l8), l32 = csq(l16);
      const v2f ci = *(const LAS v2f*)(L + L_CIN + ((ST0 + i) * 16 + c) * 8);
      C2 P = {ci.x, ci.y};
      for (int w = 0; w < tg; ++w) {
        const v2f e = *(const LAS v2f*)(L + L_EX + (((ST0 + i) * 3 + w) * 16 + c) * 8);
        P = cfma(l32, P, (C2){e.x, e.y});
      }
      C2 t = cmul(l4, P); P = h ? t : P;
      t = cmul(l16, P); P = rho ? t : P;
      C2 s[2] = {P, cmul(l8, P)};
      unsigned pr[2][2], pi[2][2];
#pragma unroll
      for (int sg = 0; sg < 2; ++sg) {
        float orr[4], oi[4];
        C2 sv = s[sg];
#pragma unroll
        for (int k = 0; k < 4; ++k) {
          sv = cfma(l1, sv, (C2){a[4 * sg + k], a[4 * sg + k + 8]});
          orr[k] = lc.z * sv.r - lc.w * sv.i;
          oi[k] = -(lc.z * sv.i + lc.w * sv.r);
        }
        pr[sg][0] = pk_bf16(orr[0], orr[1]); pr[sg][1] = pk_bf16(orr[2], orr[3]);
        pi[sg][0] = pk_bf16(oi[0], oi[1]); pi[sg][1] = pk_bf16(oi[2], oi[3]);
      }
      swap32u(pr[0][0], pr[1][0]); swap32u(pr[0][1], pr[1][1]);
      swap32u(pi[0][0], pi[1][0]); swap32u(pi[0][1], pi[1][1]);
      const unsigned off = WS_AH + (tgG * 176 + 112 + sig0 + ST0 + i) * 1024 + c * 64 + rho * 32 + h * 16;
      __builtin_amdgcn_raw_buffer_store_b128((v4u){pr[0][0], pr[0][1], pr[1][0], pr[1][1]}, wsr, off, 0, 16);
      __builtin_amdgcn_raw_buffer_store_b128((v4u){pi[0][0], pi[0][1], pi[1][0], pi[1][1]}, wsr, off + 32 * 1024, 0, 16);
    }
    asm volatile("s_waitcnt vmcnt(0)" ::: "memory");
    unsigned old = 0;
    if (lane == 0) old = lds_add_rtn(L_CNT2, 1u);
    if (rfl(old) == (SSMBLK ? 7u : 3u) && lane == 0) __hip_atomic_store((GAS unsigned*)(ws + WS_FLAG + (m1 * 16 + j1) * 128 + 4), tag, __ATOMIC_RELAXED, __HIP_MEMORY_SCOPE_AGENT);
    if (wave == 4) ST(4);
  }
}

DI bool poll_flags(GAS char* ws, int m, int word, int n, unsigned tag, int lane, Wait& W) {
  const int j = lane < n ? lane : 0;
  GAS unsigned* p = (GAS unsigned*)(ws + WS_FLAG + (m * 16 + j) * 128 + word * 4);
#pragma nounroll
  for (;;) {
    unsigned v = __hip_atomic_load(p, __ATOMIC_RELAXED, __HIP_MEMORY_SCOPE_AGENT);
    if (__all(v == tag)) return true;
    if (!W.spin()) return false;
  }
}

__global__ void __launch_bounds__(768, 3)
fused_kernel(const u16* __restrict__ x, const u16* __restrict__ Lre, const u16* __restrict__ Lim, const u16* __restrict__ Bre,
             const u16* __restrict__ Bim, const u16* __restrict__ Cre, const u16* __restrict__ Cim, const u16* __restrict__ ldt,
             const u16* __restrict__ Dsk, const u16* __restrict__ wg, const u16* __restrict__ wu, const u16* __restrict__ wd,
             const u16* __restrict__ gw, float* __restrict__ out, char* wsp) {
  extern __shared__ __attribute__((aligned(16))) char smem[];
  LAS char* L = (LAS char*)smem;
  GAS char* ws = (GAS char*)wsp;
  const int tid = threadIdx.x, lane = tid & 63, wave = rfl(tid >> 6);
  const int bid = blockIdx.x, xcd = bid & 7, idx = bid >> 3;
  const int m1 = (xcd & 1) * 8 + (idx & 7), j1 = (xcd >> 1) * 4 + (idx >> 3);
  const int rt = xcd * 4 + (idx >> 3), ct = idx & 7;
  const bool ssmblk = j1 >= 14;
  Wait W; W.budget = SPIN_CAP;
  if (wave == 0) ST(0);

  if (wave >= 8) {
    const unsigned lw = wave - 8;
    TS(ta); TS(tb); TS(tc); TS(td); TS(te); TS(tf); TS(tg_);
    const unsigned vr0 = (lane >> 3) * 1024 + (((lane & 7) ^ ((lane >> 4) & 7)) << 4);
    const unsigned vr1 = vr0 ^ 64;
    const unsigned fullw = L_FULL + lw * 4;
    {
      const unsigned c16 = lane & 15, q4 = lane >> 4;
      const unsigned vg = q4 * 3584 + (((c16 >> 1) ^ (q4 << 1)) << 5) + (c16 & 1) * 16;
      const unsigned vu = q4 * 3584 + (((c16 >> 1) ^ ((q4 << 1) | 1)) << 5) + (c16 & 1) * 16;
      const GAS char* xs = (const GAS char*)x + (size_t)(m1 * 128 + lw * 32) * 1024;
      const GAS char* gs = (const GAS char*)wg + (size_t)lw * 16 * 3584 + j1 * 256;
      const GAS char* us = (const GAS char*)wu + (size_t)lw * 16 * 3584 + j1 * 256;
      const int sig0 = ssmblk ? (14 + (j1 - 14) * 9) : j1;
      const GAS char* bs = (const GAS char*)(lw < 2 ? Bre : Bim) + (size_t)(sig0 * 16 + (lw & 1) * 8) * 1024;
      const unsigned vb = (lw & 1) ? vr1 : vr0;
      unsigned kk = ROT1 ? ((idx & 7) + 2 * (idx >> 3)) & 7 : 0;
      unsigned sbase = SLOT1;
#pragma unroll 1
      for (unsigned t = 0; t < 8; ++t) {
        if (t >= 3) W.min8w(L_PROG, t - 2);
        if (t == 0) TSET(tb);
        if (t == 1) TSET(tc);
        if (t == 4) TSET(tf);
        const GAS char* xp = xs + kk * 128;
        LAS char* lp = L + sbase + lw * 4096;
        GLDS(xp + vr0, lp, 0);
        GLDS(xp + 8192 + vr1, lp + 1024, 0);
        GLDS(xp + 16384 + vr0, lp + 2048, 0);
        GLDS(xp + 24576 + vr1, lp + 3072, 0);
        if (!ssmblk) {
          const GAS char* gp = gs + kk * (64 * 3584);
          const GAS char* up = us + kk * (64 * 3584);
#pragma unroll
          for (int p = 0; p < 4; ++p) {
            GLDS(gp + p * 4 * 3584 + vg, lp + 16384 + p * 1024, 0);
            GLDS(up + p * 4 * 3584 + vu, lp + 32768 + p * 1024, 0);
          }
          GLDS(bs + kk * 128 + vb, L + sbase + 49152 + lw * 1024, 0);
        } else {
#pragma unroll
          for (int p = 0; p < 9; ++p) GLDS(bs + p * 16384 + kk * 128 + vb, L + sbase + 16384 + p * 4096 + lw * 1024, 0);
        }
        kk = (kk + 1) & 7;
        sbase += SLOT1; if (sbase == 3 * SLOT1) sbase = 0;
        if (t == 2) {
          TSET(td);
          __builtin_amdgcn_s_barrier();
          TSET(te);
          WAIT_VM(26); lds_st(fullw, 1u);
          if (lw == 0) ST(1);
        }
        if (t == 4) TSET(tg_);
        if (t >= 2) { WAIT_VM(13); lds_st(fullw, t); }
      }
    }
    const unsigned vd = (lane >> 3) * 1024 + (((lane & 7) ^ (((lane >> 4) & 1) << 2)) << 4);
    const unsigned vl = lane * 16;
    const unsigned rotF = ROT2 ? (rt & 3) * 7 : 0, rotS = ROT2 ? (rt & 3) * 4 : 0;
    const GAS char* ah = ws + WS_AH + (size_t)(rt * 2 + (lw >> 1)) * 176 * 1024 + (lw & 1) * 2048;
    const GAS char* wds = (const GAS char*)wd + (size_t)lw * 16 * 1024 + ct * 128;
    const GAS char* crs = (const GAS char*)Cre + (size_t)(ct * 64 + lw * 16) * 1024;
    const GAS char* cis = (const GAS char*)Cim + (size_t)(ct * 64 + lw * 16) * 1024;
    unsigned ub = rotF, ua = rotF;
    unsigned db = lw * 2048, da = A2OFF + lw * 2048;
    unsigned prog = 0, npub = 8;
    auto issBF = [&]() {
      const GAS char* sp_ = wds + ub * 65536;
      GLDS(sp_ + vd, L + db, 0); GLDS(sp_ + 8192 + vd, L + db + 1024, 0);
      ub = ub == 27 ? 0 : ub + 1;
      db = db >= (RB2 - 1) * 8192 ? db - (RB2 - 1) * 8192 : db + 8192;
    };
    auto issBS = [&]() {
      const GAS char* sp_ = (ub & 8 ? cis : crs) + (ub & 7) * 128;
      GLDS(sp_ + vr0, L + db, 0); GLDS(sp_ + 8192 + vr1, L + db + 1024, 0);
      ub = (ub + 1) & 15;
      db = db >= (RB2 - 1) * 8192 ? db - (RB2 - 1) * 8192 : db + 8192;
    };
    auto issA = [&](unsigned last, unsigned first) {
      const GAS char* sp_ = ah + ua * 4096;
      GLDS(sp_ + vl, L + da, 16); GLDS(sp_ + 1024 + vl, L + da + 1024, 16);
      ua = ua == last ? first : ua + 1;
      da = da >= A2OFF + (RA2 - 1) * 8192 ? da - (RA2 - 1) * 8192 : da + 8192;
    };
    W.min8w(L_PROG, 6);
#pragma unroll 1
    for (int i = 0; i < 6; ++i) issBF();
    WAIT_VM(12); lds_st(fullw, 8u);
    if (lw == 0) ST(8);
    W.min8w(L_PROG, 7);
#pragma unroll 1
    for (int i = 0; i < 7; ++i) issBF();
    W.min8w(L_PROG, 8);
    if (lw == 0) {
      W.word(L_INIT, 8);
      const unsigned tag = rfl(lds_ld1(L_TAG));
      poll_flags(ws, rt >> 1, 0, 14, tag, lane, W);
      lds_st(L_RDY1, 1u);
      ST(5);
    } else W.word(L_RDY1, 1);
#pragma unroll 1
    for (int i = 0; i < 6; ++i) issA(27, 0);
    WAIT_VM(10); lds_st(fullw, 9u);
    WAIT_VM(8); lds_st(fullw, 10u);
    WAIT_VM(6); lds_st(fullw, 11u);
    npub = 11;
#define STEP(BISS, ALAST, AFIRST, NV) do { if (prog < npub - 2) prog = W.min4w(L_PROG, npub - 2); BISS; issA(ALAST, AFIRST); WAIT_VM(NV); ++npub; lds_st(fullw, npub); } while (0)
    STEP(issBF(), 27, 0, 8);
    STEP(issBF(), 27, 0, 10);
#pragma unroll 1
    for (int u = 2; u < 15; ++u) STEP(issBF(), 27, 0, 12);
    ub = rotS;
#pragma unroll 1
    for (int u = 15; u < 22; ++u) STEP(issBS(), 27, 0, 12);
    if (rfl(lds_ld1(L_RDY2)) == 0) {
      WAIT_VM(0); lds_st(fullw, npub + 3);
      W.word(L_RDY2, 1);
      ua = 28 + rotS;
      for (int i = 0; i < 3; ++i) { if (prog < npub - 2) prog = W.min4w(L_PROG, npub - 2); issBS(); issA(43, 28); ++npub; }
    } else {
      ua = 28 + rotS;
      for (int i = 0; i < 3; ++i) STEP(issBS(), 43, 28, 12);
    }
#pragma unroll 1
    for (int u = 25; u < 31; ++u) STEP(issBS(), 43, 28, 12);
#pragma unroll 1
    for (int u = 31; u < 38; ++u) STEP((void)0, 43, 28, 6);
    WAIT_VM(4); lds_st(fullw, 50u);
    WAIT_VM(2); lds_st(fullw, 51u);
    WAIT_VM(0); lds_st(fullw, 52u);
    if (lw == 0) { TW(9, ta); TW(10, tb); TW(11, tc); TW(12, td); TW(13, te); TW(14, tf); TW(15, tg_); }
    return;
  }

  if (tid < 64) *(LAS unsigned*)(L + MISC + tid * 4) = 0u;
  asm volatile("s_waitcnt lgkmcnt(0)" ::: "memory");
  __builtin_amdgcn_s_barrier();
  asm volatile("" ::: "memory");
  const __amdgpu_buffer_rsrc_t wsr = __builtin_amdgcn_make_buffer_rsrc((void*)wsp, 0, WS_END, 0x00020000);
  {
    const int tok = m1 * 128 + j1 * 8 + wave;
    const v4u xv = *(const GAS v4u*)((const GAS char*)x + (size_t)tok * 1024 + lane * 16);
    const v4u gv = *(const GAS v4u*)((const GAS char*)gw + lane * 16);
    if (tid == 0) *(LAS unsigned*)(L + L_TAG) = __hip_atomic_load((GAS unsigned*)(ws + WS_CTL), __ATOMIC_RELAXED, __HIP_MEMORY_SCOPE_AGENT) + 1u;
    const int nch = ssmblk ? 144 : 16;
    const int t2 = tid - 64;
    if (t2 >= 0 && t2 < nch) {
      const int p = (ssmblk ? (14 + (j1 - 14) * 9) : j1) * 16 + t2;
      const float lr = bf2f(Lre[p]), li = bf2f(Lim[p]);
      const float dt = __bfloat162float(__float2bfloat16(expf(bf2f(ldt[p]))));
      const float e = expf(dt * lr);
      float sn, cs; sincosf(dt * li, &sn, &cs);
      const float ar = e * cs, ai = e * sn;
      const float nr = ar - 1.f, den = 2.f / (lr * lr + li * li);
      *(LAS v4f*)(L + L_LAM + t2 * 16) = (v4f){ar, ai, (nr * lr + ai * li) * den, (ai * lr - nr * li) * den};
    }
    float s = 0.f;
#pragma unroll
    for (int i = 0; i < 4; ++i) { const unsigned a = xv[i], b = gv[i]; s = __builtin_amdgcn_fdot2_f32_bf16(__builtin_bit_cast(bf16x2, a), __builtin_bit_cast(bf16x2, b), s, false); }
#pragma unroll
    for (int o = 32; o >= 1; o >>= 1) s += __shfl_xor(s, o);
    const float g = 1.f / (1.f + __expf(-s));
    if (lane == 0) __hip_atomic_store((GAS unsigned*)(ws + WS_G + tok * 4), __float_as_uint(g), __ATOMIC_RELAXED, __HIP_MEMORY_SCOPE_AGENT);
    asm volatile("s_waitcnt lgkmcnt(0)" ::: "memory");
    if (lane == 0) lds_add(L_INIT, 1u);
  }
  unsigned tag;
  if (!ssmblk) {
    if (wave < 4) phase1<5, 0, 0, 0, false>(L, ws, wsr, W, wave, lane, m1, j1, tag);
    else phase1<3, 5, 1, 0, false>(L, ws, wsr, W, wave, lane, m1, j1, tag);
  } else {
    if (wave < 4) phase1<0, 0, 4, 0, true>(L, ws, wsr, W, wave, lane, m1, j1, tag);
    else phase1<0, 0, 5, 4, true>(L, ws, wsr, W, wave, lane, m1, j1, tag);
  }
  if (wave >= 4) {
    if (wave == 4) {
      const bool ok = poll_flags(ws, rt >> 1, 1, 16, tag, lane, W);
      lds_st(L_RDY2, 1u);
      ST(6);
      if (ok && bid == 255 && lane == 0) __hip_atomic_store((GAS unsigned*)(ws + WS_CTL), tag, __ATOMIC_RELAXED, __HIP_MEMORY_SCOPE_AGENT);
    }
    return;
  }
  {
    const int tq = wave & 1, dq = wave >> 1;
    const int h = lane >> 5, r32 = lane & 31;
    const int g4 = lane >> 4, q = (lane & 15) >> 2, p = lane & 3;
    const unsigned aF = tq * 4096 + lane * 16;
    const unsigned bF = (4 * (g4 >> 1) + q) * 128 + ((dq ^ (q >> 1)) << 6) + (g4 & 1) * 32 + p * 8;
    const unsigned aS = tq * 4096 + (8 * (g4 >> 1) + q) * 64 + (g4 & 1) * 32 + p * 8;
    unsigned bS[4];
#pragma unroll
    for (int ks = 0; ks < 4; ++ks) bS[ks] = (dq * 32 + r32) * 128 + (((2 * ks + h) ^ ((r32 >> 1) & 7)) << 4);
    const int tok = rt * 64 + tq * 32 + r32, d0 = ct * 64 + dq * 32 + 4 * h;
    v2u xsk[4], dsk[4];
#pragma unroll
    for (int g = 0; g < 4; ++g) {
      xsk[g] = *(const GAS v2u*)((const GAS char*)x + (size_t)tok * 1024 + (d0 + 8 * g) * 2);
      dsk[g] = *(const GAS v2u*)((const GAS char*)Dsk + (d0 + 8 * g) * 2);
    }
    f32x16 accF = {}, accS = {};
    unsigned sa = A2OFF, sbb = 0, full = 0;
    const unsigned progw = L_PROG + wave * 4;
#pragma unroll 1
    for (unsigned n = 9; n < 9 + 28; ++n) {
      if (full < n) full = W.min4w(L_FULL, n);
      bf16x8 af[4], bf[4];
#pragma unroll
      for (int ks = 0; ks < 4; ++ks) { af[ks] = ld_b128(L, sa + aF + ks * 1024); bf[ks] = ld_tr2(L, sbb + bF + ks * 2048, 1024); }
      asm volatile("s_waitcnt lgkmcnt(0)" ::: "memory");
      lds_st(progw, n);
#pragma unroll
      for (int ks = 0; ks < 4; ++ks) accF = MFMA32(bf[ks], af[ks], accF);
      sa = sa == A2OFF + (RA2 - 1) * 8192 ? A2OFF : sa + 8192;
      sbb = sbb == (RB2 - 1) * 8192 ? 0 : sbb + 8192;
    }
#pragma unroll 1
    for (unsigned n = 9 + 28; n < 9 + 44; ++n) {
      if (full < n) full = W.min4w(L_FULL, n);
      bf16x8 af[4], bf[4];
#pragma unroll
      for (int ks = 0; ks < 4; ++ks) { af[ks] = ld_tr2(L, sa + aS + ks * 1024, 256); bf[ks] = ld_b128(L, sbb + bS[ks]); }
      asm volatile("s_waitcnt lgkmcnt(0)" ::: "memory");
      lds_st(progw, n);
#pragma unroll
      for (int ks = 0; ks < 4; ++ks) accS = MFMA32(bf[ks], af[ks], accS);
      sa = sa == A2OFF + (RA2 - 1) * 8192 ? A2OFF : sa + 8192;
      sbb = sbb == (RB2 - 1) * 8192 ? 0 : sbb + 8192;
    }
    const float g = __uint_as_float(__hip_atomic_load((GAS unsigned*)(ws + WS_G + tok * 4), __ATOMIC_RELAXED, __HIP_MEMORY_SCOPE_AGENT));
    const float g1 = 1.f - g;
#pragma unroll
    for (int gq = 0; gq < 4; ++gq) {
      v4f o;
#pragma unroll
      for (int i = 0; i < 4; ++i) {
        const unsigned xw = xsk[gq][i >> 1], dw = dsk[gq][i >> 1];
        const float xv = __uint_as_float((i & 1) ? (xw & 0xffff0000u) : (xw << 16));
        const float dv = __uint_as_float((i & 1) ? (dw & 0xffff0000u) : (dw << 16));
        o[i] = g * (accS[4 * gq + i] + xv * dv) + g1 * accF[4 * gq + i];
      }
      *(GAS v4f*)((GAS char*)out + ((size_t)tok * 512 + d0 + 8 * gq) * 4) = o;
    }
    if (wave == 0) ST(7);
  }
}

extern "C" void kernel_launch(void* const* d_in, const int* in_sizes, int n_in, void* d_out, int out_size, void* d_ws, size_t ws_size, hipStream_t stream) {
  (void)in_sizes; (void)n_in; (void)out_size; (void)ws_size;
  (void)hipFuncSetAttribute((const void*)fused_kernel, hipFuncAttributeMaxDynamicSharedMemorySize, LDS_BYTES);
  fused_kernel<<<256, 768, LDS_BYTES, stream>>>((const u16*)d_in[0], (const u16*)d_in[1], (const u16*)d_in[2], (const u16*)d_in[3], (const u16*)d_in[4],
                                                (const u16*)d_in[5], (const u16*)d_in[6], (const u16*)d_in[7], (const u16*)d_in[8], (const u16*)d_in[10],
                                                (const u16*)d_in[11], (const u16*)d_in[12], (const u16*)d_in[13], (float*)d_out, (char*)d_ws);
}
```

```cpp
#include <hip/hip_runtime.h>
#include <hip/hip_bf16.h>

#define GAS __attribute__((address_space(1)))
#define LAS __attribute__((address_space(3)))
#define DI __device__ __forceinline__
typedef unsigned short u16;
typedef unsigned v4u __attribute__((ext_vector_type(4)));
typedef unsigned v2u __attribute__((ext_vector_type(2)));
typedef float v4f __attribute__((ext_vector_type(4)));
typedef float v2f __attribute__((ext_vector_type(2)));
typedef short s16x4 __attribute__((ext_vector_type(4)));
typedef short s16x8 __attribute__((ext_vector_type(8)));
typedef __bf16 bf16x8 __attribute__((ext_vector_type(8)));
typedef __bf16 bf16x2 __attribute__((ext_vector_type(2)));
typedef float f32x16 __attribute__((ext_vector_type(16)));

#define ABL 0
#define ROT1 0
#define ROT2 1

constexpr unsigned WS_CTL  = 0;
constexpr unsigned WS_FLAG = 4096;
constexpr unsigned WS_GRAN = WS_FLAG + 32768;
constexpr unsigned WS_G    = WS_GRAN + 131072;
constexpr unsigned WS_STAMP= 512u << 10;
constexpr unsigned WS_AH   = 1u << 20;
constexpr unsigned WS_END  = WS_AH + 64u * 176u * 1024u;

constexpr unsigned SLOT1 = 53248;
constexpr unsigned RB2   = 13, RA2 = 6;
constexpr unsigned A2OFF = RB2 * 8192;
constexpr unsigned L_EX   = A2OFF + RA2 * 8192;
constexpr unsigned MISC  = 3 * SLOT1;
constexpr unsigned L_FULL = MISC + 0;
constexpr unsigned L_PROG = MISC + 32;
constexpr unsigned L_RDY1 = MISC + 64;
constexpr unsigned L_RDY2 = MISC + 68;
constexpr unsigned L_INIT = MISC + 72;
constexpr unsigned L_CNT1 = MISC + 76;
constexpr unsigned L_CNT2 = MISC + 80;
constexpr unsigned L_EF   = MISC + 96;
constexpr unsigned L_CF   = MISC + 128;
constexpr unsigned L_TAG  = MISC + 256;
constexpr unsigned L_LAM  = MISC + 272;
constexpr unsigned L_CIN  = MISC + 2576;
constexpr unsigned LDS_BYTES = 163840;

constexpr unsigned SPIN_CAP = 1u << 20;

DI float bf2f(u16 v) { return __uint_as_float((unsigned)v << 16); }
DI unsigned pk_bf16(float a, float b) { v2f p = {a, b}; return __builtin_bit_cast(unsigned, __builtin_convertvector(p, bf16x2)); }
DI unsigned rfl(unsigned v) { return __builtin_amdgcn_readfirstlane(v); }

DI void lds_st(unsigned addr, unsigned v) { asm volatile("ds_write_b32 %0, %1" ::"v"(addr), "v"(v) : "memory"); }
DI v4u lds_ld4(unsigned addr) { v4u r; asm volatile("ds_read_b128 %0, %1\n\ts_waitcnt lgkmcnt(0)" : "=&v"(r) : "v"(addr) : "memory"); return r; }
DI unsigned lds_ld1(unsigned addr) { unsigned r; asm volatile("ds_read_b32 %0, %1\n\ts_waitcnt lgkmcnt(0)" : "=&v"(r) : "v"(addr) : "memory"); return r; }
DI unsigned lds_add_rtn(unsigned addr, unsigned v) { unsigned r; asm volatile("ds_add_rtn_u32 %0, %1, %2\n\ts_waitcnt lgkmcnt(0)" : "=&v"(r) : "v"(addr), "v"(v) : "memory"); return r; }
DI unsigned min4(v4u v) { unsigned a = v.x < v.y ? v.x : v.y, b = v.z < v.w ? v.z : v.w; return a < b ? a : b; }

DI void lds_add(unsigned addr, unsigned v) { asm volatile("ds_add_u32 %0, %1" ::"v"(addr), "v"(v) : "memory"); }
#define WAIT_VM(n) asm volatile("s_waitcnt vmcnt(%0)" ::"i"(n) : "memory")

struct C2 { float r, i; };
DI C2 cmul(C2 a, C2 b) { return {a.r * b.r - a.i * b.i, a.r * b.i + a.i * b.r}; }
DI C2 cfma(C2 a, C2 b, C2 c) { return {fmaf(a.r, b.r, fmaf(-a.i, b.i, c.r)), fmaf(a.r, b.i, fmaf(a.i, b.r, c.i))}; }
DI C2 csq(C2 a) { return {a.r * a.r - a.i * a.i, 2.f * a.r * a.i}; }
DI void swap32(float& x, float& y) { auto r = __builtin_amdgcn_permlane32_swap(__float_as_uint(x), __float_as_uint(y), false, false); x = __uint_as_float(r[0]); y = __uint_as_float(r[1]); }
DI void swap16(float& x, float& y) { auto r = __builtin_amdgcn_permlane16_swap(__float_as_uint(x), __float_as_uint(y), false, false); x = __uint_as_float(r[0]); y = __uint_as_float(r[1]); }
DI void swap32u(unsigned& x, unsigned& y) { auto r = __builtin_amdgcn_permlane32_swap(x, y, false, false); x = r[0]; y = r[1]; }

#define MFMA32(a, b, c) __builtin_amdgcn_mfma_f32_32x32x16_bf16((a), (b), (c), 0, 0, 0)
#define GLDS(g, l, aux) __builtin_amdgcn_global_load_lds((const GAS void*)(g), (LAS void*)(l), 16, 0, (aux))

DI bf16x8 ld_b128(LAS char* L, unsigned off) { return *(const LAS bf16x8*)(L + off); }
DI bf16x8 ld_tr2(LAS char* L, unsigned off, unsigned d) {
  s16x4 a = __builtin_amdgcn_ds_read_tr16_b64_v4i16((LAS s16x4*)(L + off));
  s16x4 b = __builtin_amdgcn_ds_read_tr16_b64_v4i16((LAS s16x4*)(L + off + d));
  return __builtin_bit_cast(bf16x8, __builtin_shufflevector(a, b, 0, 1, 2, 3, 4, 5, 6, 7));
}

#define ST(i) do {} while (0)
#define TS(v) do {} while (0)
#define TSET(v) do {} while (0)
#define TW(i, v) do {} while (0)

struct Wait {
  unsigned budget;
  DI bool spin() { __builtin_amdgcn_s_sleep(1); if (budget == 0) return false; --budget; return true; }
  DI unsigned min4w(unsigned addr, unsigned need) {
    unsigned v;
#pragma nounroll
    do { v = rfl(min4(lds_ld4(addr))); } while (v < need && spin());
    return v;
  }
  DI unsigned min8w(unsigned addr, unsigned need) {
    unsigned v;
#pragma nounroll
    do { unsigned a = min4(lds_ld4(addr)), b = min4(lds_ld4(addr + 16)); v = rfl(a < b ? a : b); } while (v < need && spin());
    return v;
  }
  DI void word(unsigned addr, unsigned need) {
#pragma nounroll
    while (rfl(lds_ld1(addr)) < need && spin()) {}
  }
  DI void four(unsigned addr, unsigned n) {
    unsigned ok;
#pragma nounroll
    do { v4u v = lds_ld4(addr); ok = rfl((n < 1 || v.x) && (n < 2 || v.y) && (n < 3 || v.z) && (n < 4 || v.w)); } while (!ok && spin());
  }
};

template <int NF, int FT0, int NS, int ST0, bool SSMBLK>
DI void phase1(LAS char* L, GAS char* ws, __amdgpu_buffer_rsrc_t wsr, Wait& W, int wave, int lane, int m1, int j1, unsigned& tag) {
  const int tg = wave & 3, grp = wave >> 2;
  const int h = lane >> 5, r32 = lane & 31;
  f32x16 accF[NF > 0 ? NF : 1], accS[NS > 0 ? NS : 1];
#pragma unroll
  for (int i = 0; i < NF; ++i) accF[i] = (f32x16){};
#pragma unroll
  for (int i = 0; i < NS; ++i) accS[i] = (f32x16){};
  unsigned ro[4];
#pragma unroll
  for (int ks = 0; ks < 4; ++ks) ro[ks] = r32 * 128 + (((2 * ks + h) ^ ((r32 >> 1) & 7)) << 4);
  unsigned to[NF > 0 ? NF : 1];
  {
    const int g = lane >> 4, img = g & 1, hh = g >> 1, q = (lane & 15) >> 2, p = lane & 3;
#pragma unroll
    for (int i = 0; i < NF; ++i) to[i] = 16384 + img * 16384 + (8 * hh + q) * 256 + (((FT0 + i) ^ ((q << 1) | img)) << 5) + p * 8;
  }
  constexpr unsigned SW0 = SSMBLK ? 16384u + ST0 * 4096u : 49152u;
  unsigned sb = SLOT1;
  TS(tw0); TS(tw1); TS(tw2); TS(tw3); TS(tw4); TS(tw5);
#pragma unroll 1
  for (int t = 0; t < 8; ++t) {
    W.min4w(L_FULL, t + 1);
    const unsigned xb = sb + tg * 4096;
#pragma unroll
    for (int ks = 0; ks < 4; ++ks) {
      bf16x8 xf = ld_b128(L, xb + ro[ks]);
      bf16x8 wf[NF > 0 ? NF : 1], sf[NS > 0 ? NS : 1];
#pragma unroll
      for (int i = 0; i < NF; ++i) wf[i] = ld_tr2(L, sb + to[i] + ks * 4096, 1024);
#pragma unroll
      for (int i = 0; i < NS; ++i) sf[i] = ld_b128(L, sb + ro[ks] + SW0 + i * 4096);
      if (ks == 3) {
        asm volatile("s_waitcnt lgkmcnt(0)" ::: "memory");
        lds_st(L_PROG + wave * 4, (unsigned)(t + 1));
      }
#pragma unroll
      for (int i = 0; i < NF; ++i) accF[i] = MFMA32(wf[i], xf, accF[i]);
#pragma unroll
      for (int i = 0; i < NS; ++i) accS[i] = MFMA32(xf, sf[i], accS[i]);
    }
    sb += SLOT1; if (sb == 3 * SLOT1) sb = 0;
  }
  if (wave == 0) ST(2);
  if (tg == 0) { TW(16 + grp * 6, tw0); TW(17 + grp * 6, tw1); TW(18 + grp * 6, tw2); TW(19 + grp * 6, tw3); TW(20 + grp * 6, tw4); TW(21 + grp * 6, tw5); }
  W.word(L_INIT, 8);
  tag = rfl(lds_ld1(L_TAG));
  const int c = lane & 15, rho = (lane >> 4) & 1;
  const unsigned tgG = m1 * 4 + tg;
  const int sig0 = SSMBLK ? (14 + (j1 - 14) * 9) : j1;
  if constexpr (NS > 0) {
    W.min8w(L_PROG, 8);
    C2 Eown[NS];
#pragma unroll
    for (int i = 0; i < NS; ++i) {
      f32x16& a = accS[i];
#pragma unroll
      for (int r = 0; r < 8; ++r) { float x = a[r], y = a[r + 8]; swap16(x, y); a[r] = x; a[r + 8] = y; }
      const v4f lc = *(const LAS v4f*)(L + L_LAM + ((ST0 + i) * 16 + c) * 16);
      const C2 l1 = {lc.x, lc.y}, l2 = csq(l1), l4 = csq(l2), l8 = csq(l4), l16 = csq(l8);
      C2 eE[2], eO[2], U[2];
#pragma unroll
      for (int sg = 0; sg < 2; ++sg) {
        C2 e = {a[4 * sg], a[4 * sg + 8]};
#pragma unroll
        for (int k = 1; k < 4; ++k) e = cfma(l1, e, (C2){a[4 * sg + k], a[4 * sg + k + 8]});
        eE[sg] = e; eO[sg] = e;
        swap32(eE[sg].r, eO[sg].r); swap32(eE[sg].i, eO[sg].i);
        U[sg] = cfma(l4, eE[sg], eO[sg]);
      }
      C2 V0 = cfma(l8, U[0], U[1]), V1 = V0;
      swap16(V0.r, V1.r); swap16(V0.i, V1.i);
      Eown[i] = cfma(l16, V0, V1);
      C2 c8[2];
      c8[0] = rho ? V0 : (C2){0.f, 0.f};
      c8[1] = cfma(l8, c8[0], U[0]);
#pragma unroll
      for (int sg = 0; sg < 2; ++sg) {
        C2 t4 = cfma(l4, c8[sg], eE[sg]);
        C2 c4 = h ? t4 : c8[sg];
        C2 u0 = cfma(l1, c4, (C2){a[4 * sg], a[4 * sg + 8]});
        a[4 * sg] = u0.r; a[4 * sg + 8] = u0.i;
      }
      if (tg < 3 && lane < 16) *(LAS v2f*)(L + L_EX + (((ST0 + i) * 3 + tg) * 16 + c) * 8) = (v2f){Eown[i].r, Eown[i].i};
      asm volatile("" : "+v"(a), "+v"(Eown[i].r), "+v"(Eown[i].i));
    }
    if (tg < 3) {
      asm volatile("s_waitcnt lgkmcnt(0)" ::: "memory");
      lds_st(L_EF + (grp * 4 + tg) * 4, 1u);
    } else if (m1 < 15) {
      W.four(L_EF + grp * 16, 3);
#pragma unroll
      for (int i = 0; i < NS; ++i) {
        const v4f lc = *(const LAS v4f*)(L + L_LAM + ((ST0 + i) * 16 + c) * 16);
        const C2 l32 = csq(csq(csq(csq(csq((C2){lc.x, lc.y})))));
        const v2f e0 = *(const LAS v2f*)(L + L_EX + (((ST0 + i) * 3 + 0) * 16 + c) * 8);
        const v2f e1 = *(const LAS v2f*)(L + L_EX + (((ST0 + i) * 3 + 1) * 16 + c) * 8);
        const v2f e2 = *(const LAS v2f*)(L + L_EX + (((ST0 + i) * 3 + 2) * 16 + c) * 8);
        C2 tot = cfma(l32, (C2){e0.x, e0.y}, (C2){e1.x, e1.y});
        tot = cfma(l32, tot, (C2){e2.x, e2.y});
        tot = cfma(l32, tot, Eown[i]);
        if (lane < 16) {
          GAS unsigned long long* gp = (GAS unsigned long long*)(ws + WS_GRAN + ((m1 * 32 + sig0 + ST0 + i) * 16 + c) * 16);
          __hip_atomic_store(gp, ((unsigned long long)tag << 32) | __float_as_uint(tot.r), __ATOMIC_RELAXED, __HIP_MEMORY_SCOPE_AGENT);
          __hip_atomic_store(gp + 1, ((unsigned long long)tag << 32) | __float_as_uint(tot.i), __ATOMIC_RELAXED, __HIP_MEMORY_SCOPE_AGENT);
        }
      }
    }
  }
  if constexpr (NF > 0) {
#pragma unroll
    for (int i = 0; i < NF; ++i) {
      float o[8];
#pragma unroll
      for (int r = 0; r < 8; ++r) {
        const float g = accF[i][r], u = accF[i][r + 8];
        const float e = __builtin_amdgcn_exp2f(g * -1.44269504088896f);
        o[r] = g * __builtin_amdgcn_rcpf(1.f + e) * u;
      }
      v4u v = {pk_bf16(o[0], o[1]), pk_bf16(o[2], o[3]), pk_bf16(o[4], o[5]), pk_bf16(o[6], o[7])};
      __builtin_amdgcn_raw_buffer_store_b128(v, wsr, WS_AH + (tgG * 176 + j1 * 8 + FT0 + i) * 1024 + lane * 16, 0, 16);
    }
    asm volatile("s_waitcnt vmcnt(0)" ::: "memory");
    unsigned old = 0;
    if (lane == 0) old = lds_add_rtn(L_CNT1, 1u);
    if (rfl(old) == 7u && lane == 0) __hip_atomic_store((GAS unsigned*)(ws + WS_FLAG + (m1 * 16 + j1) * 128), tag, __ATOMIC_RELAXED, __HIP_MEMORY_SCOPE_AGENT);
    if (wave == 0) ST(3);
  }
  if constexpr (NS > 0) {
    const int lg = lane >> 4;
#pragma unroll
    for (int i = 0; i < NS; ++i) {
      if ((i & 3) == tg) {
        C2 cin = {0.f, 0.f};
        if (m1 > 0) {
          const v4f lc = *(const LAS v4f*)(L + L_LAM + ((ST0 + i) * 16 + c) * 16);
          const C2 l128 = csq(csq(csq(csq(csq(csq(csq((C2){lc.x, lc.y}))))))), l256 = csq(l128), l512 = csq(l256), l384 = cmul(l128, l256);
          v4u gv[4];
#pragma nounroll
          for (;;) {
            asm volatile("" ::: "memory");
            bool ok = true;
#pragma unroll
            for (int k = 0; k < 4; ++k) {
              int mp = m1 - 1 - (4 * k + lg);
              int mc = mp < 0 ? 0 : mp;
              gv[k] = __builtin_amdgcn_raw_buffer_load_b128(wsr, WS_GRAN + ((mc * 32 + sig0 + ST0 + i) * 16 + c) * 16, 0, 16);
            }
#pragma unroll
            for (int k = 0; k < 4; ++k) ok = ok && gv[k].y == tag && gv[k].w == tag;
            if (__all(ok) || !W.spin()) break;
          }
          C2 acc = {0.f, 0.f};
#pragma unroll
          for (int k = 3; k >= 0; --k) {
            const bool valid = (m1 - 1 - (4 * k + lg)) >= 0;
            C2 e = {valid ? __uint_as_float(gv[k].x) : 0.f, valid ? __uint_as_float(gv[k].z) : 0.f};
            acc = cfma(l512, acc, e);
          }
          C2 wgt = lg == 0 ? (C2){1.f, 0.f} : lg == 1 ? l128 : lg == 2 ? l256 : l384;
          acc = cmul(acc, wgt);
          C2 o = acc; swap16(acc.r, o.r); swap16(acc.i, o.i); acc.r += o.r; acc.i += o.i;
          o = acc; swap32(acc.r, o.r); swap32(acc.i, o.i); acc.r += o.r; acc.i += o.i;
          cin = acc;
        }
        if (lane < 16) *(LAS v2f*)(L + L_CIN + ((ST0 + i) * 16 + c) * 8) = (v2f){cin.r, cin.i};
      }
    }
    asm volatile("s_waitcnt lgkmcnt(0)" ::: "memory");
    lds_st(L_CF + (grp * 4 + tg) * 4, 1u);
    W.four(L_CF + grp * 16, NS < 4 ? NS : 4);
    W.four(L_EF + grp * 16, tg);
#pragma unroll
    for (int i = 0; i < NS; ++i) {
      f32x16& a = accS[i];
      const v4f lc = *(const LAS v4f*)(L + L_LAM + ((ST0 + i) * 16 + c) * 16);
      const C2 l1 = {lc.x, lc.y}, l2 = csq(l1), l4 = csq(l2), l8 = csq(l4), l16 = csq(l8), l32 = csq(l16);
      const v2f ci = *(const LAS v2f*)(L + L_CIN + ((ST0 + i) * 16 + c) * 8);
      C2 P = {ci.x, ci.y};
      for (int w = 0; w < tg; ++w) {
        const v2f e = *(const LAS v2f*)(L + L_EX + (((ST0 + i) * 3 + w) * 16 + c) * 8);
        P = cfma(l32, P, (C2){e.x, e.y});
      }
      C2 t = cmul(l4, P); P = h ? t : P;
      t = cmul(l16, P); P = rho ? t : P;
      C2 s[2] = {P, cmul(l8, P)};
      unsigned pr[2][2], pi[2][2];
#pragma unroll
      for (int sg = 0; sg < 2; ++sg) {
        float orr[4], oi[4];
        C2 sv = s[sg];
#pragma unroll
        for (int k = 0; k < 4; ++k) {
          sv = cfma(l1, sv, (C2){a[4 * sg + k], a[4 * sg + k + 8]});
          orr[k] = lc.z * sv.r - lc.w * sv.i;
          oi[k] = -(lc.z * sv.i + lc.w * sv.r);
        }
        pr[sg][0] = pk_bf16(orr[0], orr[1]); pr[sg][1] = pk_bf16(orr[2], orr[3]);
        pi[sg][0] = pk_bf16(oi[0], oi[1]); pi[sg][1] = pk_bf16(oi[2], oi[3]);
      }
      swap32u(pr[0][0], pr[1][0]); swap32u(pr[0][1], pr[1][1]);
      swap32u(pi[0][0], pi[1][0]); swap32u(pi[0][1], pi[1][1]);
      const unsigned off = WS_AH + (tgG * 176 + 112 + sig0 + ST0 + i) * 1024 + c * 64 + rho * 32 + h * 16;
      __builtin_amdgcn_raw_buffer_store_b128((v4u){pr[0][0], pr[0][1], pr[1][0], pr[1][1]}, wsr, off, 0, 16);
      __builtin_amdgcn_raw_buffer_store_b128((v4u){pi[0][0], pi[0][1], pi[1][0], pi[1][1]}, wsr, off + 32 * 1024, 0, 16);
    }
    asm volatile("s_waitcnt vmcnt(0)" ::: "memory");
    unsigned old = 0;
    if (lane == 0) old = lds_add_rtn(L_CNT2, 1u);
    if (rfl(old) == (SSMBLK ? 7u : 3u) && lane == 0) __hip_atomic_store((GAS unsigned*)(ws + WS_FLAG + (m1 * 16 + j1) * 128 + 4), tag, __ATOMIC_RELAXED, __HIP_MEMORY_SCOPE_AGENT);
    if (wave == 4) ST(4);
  }
}

DI bool poll_flags(GAS char* ws, int m, int word, int n, unsigned tag, int lane, Wait& W) {
  const int j = lane < n ? lane : 0;
  GAS unsigned* p = (GAS unsigned*)(ws + WS_FLAG + (m * 16 + j) * 128 + word * 4);
#pragma nounroll
  for (;;) {
    unsigned v = __hip_atomic_load(p, __ATOMIC_RELAXED, __HIP_MEMORY_SCOPE_AGENT);
    if (__all(v == tag)) return true;
    if (!W.spin()) return false;
  }
}

extern "C" __global__ void __launch_bounds__(768, 3)
fused_kernel(const u16* __restrict__ x, const u16* __restrict__ Lre, const u16* __restrict__ Lim, const u16* __restrict__ Bre,
             const u16* __restrict__ Bim, const u16* __restrict__ Cre, const u16* __restrict__ Cim, const u16* __restrict__ ldt,
             const u16* __restrict__ Dsk, const u16* __restrict__ wg, const u16* __restrict__ wu, const u16* __restrict__ wd,
             const u16* __restrict__ gw, float* __restrict__ out, char* wsp) {
  extern __shared__ __attribute__((aligned(16))) char smem[];
  LAS char* L = (LAS char*)smem;
  GAS char* ws = (GAS char*)wsp;
  const int tid = threadIdx.x, lane = tid & 63, wave = rfl(tid >> 6);
  const int bid = blockIdx.x, xcd = bid & 7, idx = bid >> 3;
  const int m1 = (xcd & 1) * 8 + (idx & 7), j1 = (xcd >> 1) * 4 + (idx >> 3);
  const int rt = xcd * 4 + (idx >> 3), ct = idx & 7;
  const bool ssmblk = j1 >= 14;
  Wait W; W.budget = SPIN_CAP;
  if (wave == 0) ST(0);

  if (wave >= 8) {
    const unsigned lw = wave - 8;
    TS(ta); TS(tb); TS(tc); TS(td); TS(te); TS(tf); TS(tg_);
    const unsigned vr0 = (lane >> 3) * 1024 + (((lane & 7) ^ ((lane >> 4) & 7)) << 4);
    const unsigned vr1 = vr0 ^ 64;
    const unsigned fullw = L_FULL + lw * 4;
    {
      const unsigned c16 = lane & 15, q4 = lane >> 4;
      const unsigned vg = q4 * 3584 + (((c16 >> 1) ^ (q4 << 1)) << 5) + (c16 & 1) * 16;
      const unsigned vu = q4 * 3584 + (((c16 >> 1) ^ ((q4 << 1) | 1)) << 5) + (c16 & 1) * 16;
      const GAS char* xs = (const GAS char*)x + (size_t)(m1 * 128 + lw * 32) * 1024;
      const GAS char* gs = (const GAS char*)wg + (size_t)lw * 16 * 3584 + j1 * 256;
      const GAS char* us = (const GAS char*)wu + (size_t)lw * 16 * 3584 + j1 * 256;
      const int sig0 = ssmblk ? (14 + (j1 - 14) * 9) : j1;
      const GAS char* bs = (const GAS char*)(lw < 2 ? Bre : Bim) + (size_t)(sig0 * 16 + (lw & 1) * 8) * 1024;
      const unsigned vb = (lw & 1) ? vr1 : vr0;
      unsigned kk = ROT1 ? ((idx & 7) + 2 * (idx >> 3)) & 7 : 0;
      unsigned sbase = SLOT1;
#pragma unroll 1
      for (unsigned t = 0; t < 8; ++t) {
        if (t >= 3) W.min8w(L_PROG, t - 2);
        if (t == 0) TSET(tb);
        if (t == 1) TSET(tc);
        if (t == 4) TSET(tf);
        const GAS char* xp = xs + kk * 128;
        LAS char* lp = L + sbase + lw * 4096;
        GLDS(xp + vr0, lp, 0);
        GLDS(xp + 8192 + vr1, lp + 1024, 0);
        GLDS(xp + 16384 + vr0, lp + 2048, 0);
        GLDS(xp + 24576 + vr1, lp + 3072, 0);
        if (!ssmblk) {
          const GAS char* gp = gs + kk * (64 * 3584);
          const GAS char* up = us + kk * (64 * 3584);
#pragma unroll
          for (int p = 0; p < 4; ++p) {
            GLDS(gp + p * 4 * 3584 + vg, lp + 16384 + p * 1024, 0);
            GLDS(up + p * 4 * 3584 + vu, lp + 32768 + p * 1024, 0);
          }
          GLDS(bs + kk * 128 + vb, L + sbase + 49152 + lw * 1024, 0);
        } else {
#pragma unroll
          for (int p = 0; p < 9; ++p) GLDS(bs + p * 16384 + kk * 128 + vb, L + sbase + 16384 + p * 4096 + lw * 1024, 0);
        }
        kk = (kk + 1) & 7;
        sbase += SLOT1; if (sbase == 3 * SLOT1) sbase = 0;
        if (t == 2) {
          TSET(td);
          __builtin_amdgcn_s_barrier();
          TSET(te);
          WAIT_VM(26); lds_st(fullw, 1u);
          if (lw == 0) ST(1);
        }
        if (t == 4) TSET(tg_);
        if (t >= 2) { WAIT_VM(13); lds_st(fullw, t); }
      }
    }
    const unsigned vd = (lane >> 3) * 1024 + (((lane & 7) ^ (((lane >> 4) & 1) << 2)) << 4);
    const unsigned vl = lane * 16;
    const unsigned rotF = ROT2 ? (rt & 3) * 7 : 0, rotS = ROT2 ? (rt & 3) * 4 : 0;
    const GAS char* ah = ws + WS_AH + (size_t)(rt * 2 + (lw >> 1)) * 176 * 1024 + (lw & 1) * 2048;
    const GAS char* wds = (const GAS char*)wd + (size_t)lw * 16 * 1024 + ct * 128;
    const GAS char* crs = (const GAS char*)Cre + (size_t)(ct * 64 + lw * 16) * 1024;
    const GAS char* cis = (const GAS char*)Cim + (size_t)(ct * 64 + lw * 16) * 1024;
    unsigned ub = rotF, ua = rotF;
    unsigned db = lw * 2048, da = A2OFF + lw * 2048;
    unsigned prog = 0, npub = 8;
    auto issBF = [&]() {
      const GAS char* sp_ = wds + ub * 65536;
      GLDS(sp_ + vd, L + db, 0); GLDS(sp_ + 8192 + vd, L + db + 1024, 0);
      ub = ub == 27 ? 0 : ub + 1;
      db = db >= (RB2 - 1) * 8192 ? db - (RB2 - 1) * 8192 : db + 8192;
    };
    auto issBS = [&]() {
      const GAS char* sp_ = (ub & 8 ? cis : crs) + (ub & 7) * 128;
      GLDS(sp_ + vr0, L + db, 0); GLDS(sp_ + 8192 + vr1, L + db + 1024, 0);
      ub = (ub + 1) & 15;
      db = db >= (RB2 - 1) * 8192 ? db - (RB2 - 1) * 8192 : db + 8192;
    };
    auto issA = [&](unsigned last, unsigned first) {
      const GAS char* sp_ = ah + ua * 4096;
      GLDS(sp_ + vl, L + da, 16); GLDS(sp_ + 1024 + vl, L + da + 1024, 16);
      ua = ua == last ? first : ua + 1;
      da = da >= A2OFF + (RA2 - 1) * 8192 ? da - (RA2 - 1) * 8192 : da + 8192;
    };
    W.min8w(L_PROG, 6);
#pragma unroll 1
    for (int i = 0; i < 6; ++i) issBF();
    WAIT_VM(12); lds_st(fullw, 8u);
    if (lw == 0) ST(8);
    W.min8w(L_PROG, 7);
#pragma unroll 1
    for (int i = 0; i < 7; ++i) issBF();
    W.min8w(L_PROG, 8);
    if (lw == 0) {
      W.word(L_INIT, 8);
      const unsigned tag = rfl(lds_ld1(L_TAG));
      poll_flags(ws, rt >> 1, 0, 14, tag, lane, W);
      lds_st(L_RDY1, 1u);
      ST(5);
    } else W.word(L_RDY1, 1);
#pragma unroll 1
    for (int i = 0; i < 6; ++i) issA(27, 0);
    WAIT_VM(10); lds_st(fullw, 9u);
    WAIT_VM(8); lds_st(fullw, 10u);
    WAIT_VM(6); lds_st(fullw, 11u);
    npub = 11;
#define STEP(BISS, ALAST, AFIRST, NV) do { if (prog < npub - 2) prog = W.min4w(L_PROG, npub - 2); BISS; issA(ALAST, AFIRST); WAIT_VM(NV); ++npub; lds_st(fullw, npub); } while (0)
    STEP(issBF(), 27, 0, 8);
    STEP(issBF(), 27, 0, 10);
#pragma unroll 1
    for (int u = 2; u < 15; ++u) STEP(issBF(), 27, 0, 12);
    ub = rotS;
#pragma unroll 1
    for (int u = 15; u < 22; ++u) STEP(issBS(), 27, 0, 12);
    if (rfl(lds_ld1(L_RDY2)) == 0) {
      WAIT_VM(0); lds_st(fullw, npub + 3);
      W.word(L_RDY2, 1);
      ua = 28 + rotS;
      for (int i = 0; i < 3; ++i) { if (prog < npub - 2) prog = W.min4w(L_PROG, npub - 2); issBS(); issA(43, 28); ++npub; }
    } else {
      ua = 28 + rotS;
      for (int i = 0; i < 3; ++i) STEP(issBS(), 43, 28, 12);
    }
#pragma unroll 1
    for (int u = 25; u < 31; ++u) STEP(issBS(), 43, 28, 12);
#pragma unroll 1
    for (int u = 31; u < 38; ++u) STEP((void)0, 43, 28, 6);
    WAIT_VM(4); lds_st(fullw, 50u);
    WAIT_VM(2); lds_st(fullw, 51u);
    WAIT_VM(0); lds_st(fullw, 52u);
    if (lw == 0) { TW(9, ta); TW(10, tb); TW(11, tc); TW(12, td); TW(13, te); TW(14, tf); TW(15, tg_); }
    return;
  }

  unsigned cpf;
  {
    unsigned long long pc; unsigned len, here;
    asm volatile("s_getpc_b64 %0\n.Lpf_here:\n\ts_mov_b32 %1, 0xc700\n\ts_mov_b32 %2, .Lpf_here-fused_kernel" : "=s"(pc), "=s"(len), "=s"(here));
    unsigned off = tid * 128; off = off < len - 4 ? off : 0;
    cpf = *(const GAS unsigned*)(pc - here + off);
  }
  if (tid < 64) *(LAS unsigned*)(L + MISC + tid * 4) = 0u;
  asm volatile("s_waitcnt lgkmcnt(0)" ::: "memory");
  __builtin_amdgcn_s_barrier();
  asm volatile("" ::: "memory");
  const __amdgpu_buffer_rsrc_t wsr = __builtin_amdgcn_make_buffer_rsrc((void*)wsp, 0, WS_END, 0x00020000);
  {
    const int tok = m1 * 128 + j1 * 8 + wave;
    const v4u xv = *(const GAS v4u*)((const GAS char*)x + (size_t)tok * 1024 + lane * 16);
    const v4u gv = *(const GAS v4u*)((const GAS char*)gw + lane * 16);
    if (tid == 0) *(LAS unsigned*)(L + L_TAG) = __hip_atomic_load((GAS unsigned*)(ws + WS_CTL), __ATOMIC_RELAXED, __HIP_MEMORY_SCOPE_AGENT) + 1u;
    const int nch = ssmblk ? 144 : 16;
    const int t2 = tid - 64;
    if (t2 >= 0 && t2 < nch) {
      const int p = (ssmblk ? (14 + (j1 - 14) * 9) : j1) * 16 + t2;
      const float lr = bf2f(Lre[p]), li = bf2f(Lim[p]);
      const float dt = __bfloat162float(__float2bfloat16(expf(bf2f(ldt[p]))));
      const float e = expf(dt * lr);
      float sn, cs; sincosf(dt * li, &sn, &cs);
      const float ar = e * cs, ai = e * sn;
      const float nr = ar - 1.f, den = 2.f / (lr * lr + li * li);
      *(LAS v4f*)(L + L_LAM + t2 * 16) = (v4f){ar, ai, (nr * lr + ai * li) * den, (ai * lr - nr * li) * den};
    }
    float s = 0.f;
#pragma unroll
    for (int i = 0; i < 4; ++i) { const unsigned a = xv[i], b = gv[i]; s = __builtin_amdgcn_fdot2_f32_bf16(__builtin_bit_cast(bf16x2, a), __builtin_bit_cast(bf16x2, b), s, false); }
#pragma unroll
    for (int o = 32; o >= 1; o >>= 1) s += __shfl_xor(s, o);
    const float g = 1.f / (1.f + __expf(-s));
    if (lane == 0) __hip_atomic_store((GAS unsigned*)(ws + WS_G + tok * 4), __float_as_uint(g), __ATOMIC_RELAXED, __HIP_MEMORY_SCOPE_AGENT);
    asm volatile("s_waitcnt lgkmcnt(0)" ::: "memory");
    asm volatile("" :: "v"(cpf));
    if (lane == 0) lds_add(L_INIT, 1u);
  }
  unsigned tag;
  if (!ssmblk) {
    if (wave < 4) phase1<5, 0, 0, 0, false>(L, ws, wsr, W, wave, lane, m1, j1, tag);
    else phase1<3, 5, 1, 0, false>(L, ws, wsr, W, wave, lane, m1, j1, tag);
  } else {
    if (wave < 4) phase1<0, 0, 4, 0, true>(L, ws, wsr, W, wave, lane, m1, j1, tag);
    else phase1<0, 0, 5, 4, true>(L, ws, wsr, W, wave, lane, m1, j1, tag);
  }
  if (wave >= 4) {
    if (wave == 4) {
      const bool ok = poll_flags(ws, rt >> 1, 1, 16, tag, lane, W);
      lds_st(L_RDY2, 1u);
      ST(6);
      if (ok && bid == 255 && lane == 0) __hip_atomic_store((GAS unsigned*)(ws + WS_CTL), tag, __ATOMIC_RELAXED, __HIP_MEMORY_SCOPE_AGENT);
    }
    return;
  }
  {
    const int tq = wave & 1, dq = wave >> 1;
    const int h = lane >> 5, r32 = lane & 31;
    const int g4 = lane >> 4, q = (lane & 15) >> 2, p = lane & 3;
    const unsigned aF = tq * 4096 + lane * 16;
    const unsigned bF = (4 * (g4 >> 1) + q) * 128 + ((dq ^ (q >> 1)) << 6) + (g4 & 1) * 32 + p * 8;
    const unsigned aS = tq * 4096 + (8 * (g4 >> 1) + q) * 64 + (g4 & 1) * 32 + p * 8;
    unsigned bS[4];
#pragma unroll
    for (int ks = 0; ks < 4; ++ks) bS[ks] = (dq * 32 + r32) * 128 + (((2 * ks + h) ^ ((r32 >> 1) & 7)) << 4);
    const int tok = rt * 64 + tq * 32 + r32, d0 = ct * 64 + dq * 32 + 4 * h;
    v2u xsk[4], dsk[4];
#pragma unroll
    for (int g = 0; g < 4; ++g) {
      xsk[g] = *(const GAS v2u*)((const GAS char*)x + (size_t)tok * 1024 + (d0 + 8 * g) * 2);
      dsk[g] = *(const GAS v2u*)((const GAS char*)Dsk + (d0 + 8 * g) * 2);
    }
    f32x16 accF = {}, accS = {};
    unsigned sa = A2OFF, sbb = 0, full = 0;
    const unsigned progw = L_PROG + wave * 4;
#pragma unroll 1
    for (unsigned n = 9; n < 9 + 28; ++n) {
      if (full < n) full = W.min4w(L_FULL, n);
      bf16x8 af[4], bf[4];
#pragma unroll
      for (int ks = 0; ks < 4; ++ks) { af[ks] = ld_b128(L, sa + aF + ks * 1024); bf[ks] = ld_tr2(L, sbb + bF + ks * 2048, 1024); }
      asm volatile("s_waitcnt lgkmcnt(0)" ::: "memory");
      lds_st(progw, n);
#pragma unroll
      for (int ks = 0; ks < 4; ++ks) accF = MFMA32(bf[ks], af[ks], accF);
      sa = sa == A2OFF + (RA2 - 1) * 8192 ? A2OFF : sa + 8192;
      sbb = sbb == (RB2 - 1) * 8192 ? 0 : sbb + 8192;
    }
#pragma unroll 1
    for (unsigned n = 9 + 28; n < 9 + 44; ++n) {
      if (full < n) full = W.min4w(L_FULL, n);
      bf16x8 af[4], bf[4];
#pragma unroll
      for (int ks = 0; ks < 4; ++ks) { af[ks] = ld_tr2(L, sa + aS + ks * 1024, 256); bf[ks] = ld_b128(L, sbb + bS[ks]); }
      asm volatile("s_waitcnt lgkmcnt(0)" ::: "memory");
      lds_st(progw, n);
#pragma unroll
      for (int ks = 0; ks < 4; ++ks) accS = MFMA32(bf[ks], af[ks], accS);
      sa = sa == A2OFF + (RA2 - 1) * 8192 ? A2OFF : sa + 8192;
      sbb = sbb == (RB2 - 1) * 8192 ? 0 : sbb + 8192;
    }
    const float g = __uint_as_float(__hip_atomic_load((GAS unsigned*)(ws + WS_G + tok * 4), __ATOMIC_RELAXED, __HIP_MEMORY_SCOPE_AGENT));
    const float g1 = 1.f - g;
#pragma unroll
    for (int gq = 0; gq < 4; ++gq) {
      v4f o;
#pragma unroll
      for (int i = 0; i < 4; ++i) {
        const unsigned xw = xsk[gq][i >> 1], dw = dsk[gq][i >> 1];
        const float xv = __uint_as_float((i & 1) ? (xw & 0xffff0000u) : (xw << 16));
        const float dv = __uint_as_float((i & 1) ? (dw & 0xffff0000u) : (dw << 16));
        o[i] = g * (accS[4 * gq + i] + xv * dv) + g1 * accF[4 * gq + i];
      }
      *(GAS v4f*)((GAS char*)out + ((size_t)tok * 512 + d0 + 8 * gq) * 4) = o;
    }
    if (wave == 0) ST(7);
  }
}

extern "C" void kernel_launch(void* const* d_in, const int* in_sizes, int n_in, void* d_out, int out_size, void* d_ws, size_t ws_size, hipStream_t stream) {
  (void)in_sizes; (void)n_in; (void)out_size; (void)ws_size;
  (void)hipFuncSetAttribute((const void*)fused_kernel, hipFuncAttributeMaxDynamicSharedMemorySize, LDS_BYTES);
  fused_kernel<<<256, 768, LDS_BYTES, stream>>>((const u16*)d_in[0], (const u16*)d_in[1], (const u16*)d_in[2], (const u16*)d_in[3], (const u16*)d_in[4],
                                                (const u16*)d_in[5], (const u16*)d_in[6], (const u16*)d_in[7], (const u16*)d_in[8], (const u16*)d_in[10],
                                                (const u16*)d_in[11], (const u16*)d_in[12], (const u16*)d_in[13], (float*)d_out, (char*)d_ws);
}
```

```cpp
#include <hip/hip_runtime.h>
#include <hip/hip_bf16.h>

#define GAS __attribute__((address_space(1)))
#define LAS __attribute__((address_space(3)))
#define DI __device__ __forceinline__
typedef unsigned short u16;
typedef unsigned v4u __attribute__((ext_vector_type(4)));
typedef unsigned v2u __attribute__((ext_vector_type(2)));
typedef float v4f __attribute__((ext_vector_type(4)));
typedef float v2f __attribute__((ext_vector_type(2)));
typedef short s16x4 __attribute__((ext_vector_type(4)));
typedef short s16x8 __attribute__((ext_vector_type(8)));
typedef __bf16 bf16x8 __attribute__((ext_vector_type(8)));
typedef __bf16 bf16x2 __attribute__((ext_vector_type(2)));
typedef float f32x16 __attribute__((ext_vector_type(16)));

#define ABL 0
#define ROT1 0
#define ROT2 1

constexpr unsigned WS_CTL  = 0;
constexpr unsigned WS_FLAG = 4096;
constexpr unsigned WS_GRAN = WS_FLAG + 32768;
constexpr unsigned WS_G    = WS_GRAN + 131072;
constexpr unsigned WS_STAMP= 512u << 10;
constexpr unsigned WS_AH   = 1u << 20;
constexpr unsigned WS_END  = WS_AH + 64u * 176u * 1024u;

constexpr unsigned SLOT1 = 53248;
constexpr unsigned RB2   = 13, RA2 = 6;
constexpr unsigned A2OFF = RB2 * 8192;
constexpr unsigned L_EX   = A2OFF + RA2 * 8192;
constexpr unsigned MISC  = 3 * SLOT1;
constexpr unsigned L_FULL = MISC + 0;
constexpr unsigned L_PROG = MISC + 32;
constexpr unsigned L_RDY1 = MISC + 64;
constexpr unsigned L_RDY2 = MISC + 68;
constexpr unsigned L_INIT = MISC + 72;
constexpr unsigned L_CNT1 = MISC + 76;
constexpr unsigned L_CNT2 = MISC + 80;
constexpr unsigned L_EF   = MISC + 96;
constexpr unsigned L_CF   = MISC + 128;
constexpr unsigned L_TAG  = MISC + 256;
constexpr unsigned L_LAM  = MISC + 272;
constexpr unsigned L_CIN  = MISC + 2576;
constexpr unsigned LDS_BYTES = 163840;

constexpr unsigned SPIN_CAP = 1u << 20;

DI float bf2f(u16 v) { return __uint_as_float((unsigned)v << 16); }
DI unsigned pk_bf16(float a, float b) { v2f p = {a, b}; return __builtin_bit_cast(unsigned, __builtin_convertvector(p, bf16x2)); }
DI unsigned rfl(unsigned v) { return __builtin_amdgcn_readfirstlane(v); }

DI void lds_st(unsigned addr, unsigned v) { asm volatile("ds_write_b32 %0, %1" ::"v"(addr), "v"(v) : "memory"); }
DI v4u lds_ld4(unsigned addr) { v4u r; asm volatile("ds_read_b128 %0, %1\n\ts_waitcnt lgkmcnt(0)" : "=&v"(r) : "v"(addr) : "memory"); return r; }
DI unsigned lds_ld1(unsigned addr) { unsigned r; asm volatile("ds_read_b32 %0, %1\n\ts_waitcnt lgkmcnt(0)" : "=&v"(r) : "v"(addr) : "memory"); return r; }
DI unsigned lds_add_rtn(unsigned addr, unsigned v) { unsigned r; asm volatile("ds_add_rtn_u32 %0, %1, %2\n\ts_waitcnt lgkmcnt(0)" : "=&v"(r) : "v"(addr), "v"(v) : "memory"); return r; }
DI unsigned min4(v4u v) { unsigned a = v.x < v.y ? v.x : v.y, b = v.z < v.w ? v.z : v.w; return a < b ? a : b; }

DI void lds_add(unsigned addr, unsigned v) { asm volatile("ds_add_u32 %0, %1" ::"v"(addr), "v"(v) : "memory"); }
#define WAIT_VM(n) asm volatile("s_waitcnt vmcnt(%0)" ::"i"(n) : "memory")

struct C2 { float r, i; };
DI C2 cmul(C2 a, C2 b) { return {a.r * b.r - a.i * b.i, a.r * b.i + a.i * b.r}; }
DI C2 cfma(C2 a, C2 b, C2 c) { return {fmaf(a.r, b.r, fmaf(-a.i, b.i, c.r)), fmaf(a.r, b.i, fmaf(a.i, b.r, c.i))}; }
DI C2 csq(C2 a) { return {a.r * a.r - a.i * a.i, 2.f * a.r * a.i}; }
DI void swap32(float& x, float& y) { auto r = __builtin_amdgcn_permlane32_swap(__float_as_uint(x), __float_as_uint(y), false, false); x = __uint_as_float(r[0]); y = __uint_as_float(r[1]); }
DI void swap16(float& x, float& y) { auto r = __builtin_amdgcn_permlane16_swap(__float_as_uint(x), __float_as_uint(y), false, false); x = __uint_as_float(r[0]); y = __uint_as_float(r[1]); }
DI void swap32u(unsigned& x, unsigned& y) { auto r = __builtin_amdgcn_permlane32_swap(x, y, false, false); x = r[0]; y = r[1]; }

#define MFMA32(a, b, c) __builtin_amdgcn_mfma_f32_32x32x16_bf16((a), (b), (c), 0, 0, 0)
#define GLDS(g, l, aux) __builtin_amdgcn_global_load_lds((const GAS void*)(g), (LAS void*)(l), 16, 0, (aux))

DI bf16x8 ld_b128(LAS char* L, unsigned off) { return *(const LAS bf16x8*)(L + off); }
DI bf16x8 ld_tr2(LAS char* L, unsigned off, unsigned d) {
  s16x4 a = __builtin_amdgcn_ds_read_tr16_b64_v4i16((LAS s16x4*)(L + off));
  s16x4 b = __builtin_amdgcn_ds_read_tr16_b64_v4i16((LAS s16x4*)(L + off + d));
  return __builtin_bit_cast(bf16x8, __builtin_shufflevector(a, b, 0, 1, 2, 3, 4, 5, 6, 7));
}

#define ST(i) do {} while (0)
#define TS(v) do {} while (0)
#define TSET(v) do {} while (0)
#define TW(i, v) do {} while (0)

struct Wait {
  unsigned budget;
  DI bool spin() { __builtin_amdgcn_s_sleep(1); if (budget == 0) return false; --budget; return true; }
  DI unsigned min4w(unsigned addr, unsigned need) {
    unsigned v;
#pragma nounroll
    do { v = rfl(min4(lds_ld4(addr))); } while (v < need && spin());
    return v;
  }
  DI unsigned min8w(unsigned addr, unsigned need) {
    unsigned v;
#pragma nounroll
    do { unsigned a = min4(lds_ld4(addr)), b = min4(lds_ld4(addr + 16)); v = rfl(a < b ? a : b); } while (v < need && spin());
    return v;
  }
  DI void word(unsigned addr, unsigned need) {
#pragma nounroll
    while (rfl(lds_ld1(addr)) < need && spin()) {}
  }
  DI void four(unsigned addr, unsigned n) {
    unsigned ok;
#pragma nounroll
    do { v4u v = lds_ld4(addr); ok = rfl((n < 1 || v.x) && (n < 2 || v.y) && (n < 3 || v.z) && (n < 4 || v.w)); } while (!ok && spin());
  }
};

template <int NF, int FT0, int NS, int ST0, bool SSMBLK>
DI void phase1(LAS char* L, GAS char* ws, __amdgpu_buffer_rsrc_t wsr, Wait& W, int wave, int lane, int m1, int j1, unsigned& tag) {
  const int tg = wave & 3, grp = wave >> 2;
  const int h = lane >> 5, r32 = lane & 31;
  f32x16 accF[NF > 0 ? NF : 1], accS[NS > 0 ? NS : 1];
#pragma unroll
  for (int i = 0; i < NF; ++i) accF[i] = (f32x16){};
#pragma unroll
  for (int i = 0; i < NS; ++i) accS[i] = (f32x16){};
  unsigned ro[4];
#pragma unroll
  for (int ks = 0; ks < 4; ++ks) ro[ks] = r32 * 128 + (((2 * ks + h) ^ ((r32 >> 1) & 7)) << 4);
  unsigned to[NF > 0 ? NF : 1];
  {
    const int g = lane >> 4, img = g & 1, hh = g >> 1, q = (lane & 15) >> 2, p = lane & 3;
#pragma unroll
    for (int i = 0; i < NF; ++i) to[i] = 16384 + img * 16384 + (8 * hh + q) * 256 + (((FT0 + i) ^ ((q << 1) | img)) << 5) + p * 8;
  }
  constexpr unsigned SW0 = SSMBLK ? 16384u + ST0 * 4096u : 49152u;
  unsigned sb = SLOT1;
  TS(tw0); TS(tw1); TS(tw2); TS(tw3); TS(tw4); TS(tw5);
#pragma unroll 1
  for (int t = 0; t < 8; ++t) {
    W.min4w(L_FULL, t + 1);
    const unsigned xb = sb + tg * 4096;
#pragma unroll
    for (int ks = 0; ks < 4; ++ks) {
      bf16x8 xf = ld_b128(L, xb + ro[ks]);
      bf16x8 wf[NF > 0 ? NF : 1], sf[NS > 0 ? NS : 1];
#pragma unroll
      for (int i = 0; i < NF; ++i) wf[i] = ld_tr2(L, sb + to[i] + ks * 4096, 1024);
#pragma unroll
      for (int i = 0; i < NS; ++i) sf[i] = ld_b128(L, sb + ro[ks] + SW0 + i * 4096);
      if (ks == 3) {
        asm volatile("s_waitcnt lgkmcnt(0)" ::: "memory");
        lds_st(L_PROG + wave * 4, (unsigned)(t + 1));
      }
#pragma unroll
      for (int i = 0; i < NF; ++i) accF[i] = MFMA32(wf[i], xf, accF[i]);
#pragma unroll
      for (int i = 0; i < NS; ++i) accS[i] = MFMA32(xf, sf[i], accS[i]);
    }
    sb += SLOT1; if (sb == 3 * SLOT1) sb = 0;
  }
  if (wave == 0) ST(2);
  if (tg == 0) { TW(16 + grp * 6, tw0); TW(17 + grp * 6, tw1); TW(18 + grp * 6, tw2); TW(19 + grp * 6, tw3); TW(20 + grp * 6, tw4); TW(21 + grp * 6, tw5); }
  W.word(L_INIT, 8);
  tag = rfl(lds_ld1(L_TAG));
  const int c = lane & 15, rho = (lane >> 4) & 1;
  const unsigned tgG = m1 * 4 + tg;
  const int sig0 = SSMBLK ? (14 + (j1 - 14) * 9) : j1;
  if constexpr (NS > 0) {
    W.min8w(L_PROG, 8);
    C2 Eown[NS];
#pragma unroll
    for (int i = 0; i < NS; ++i) {
      f32x16& a = accS[i];
#pragma unroll
      for (int r = 0; r < 8; ++r) { float x = a[r], y = a[r + 8]; swap16(x, y); a[r] = x; a[r + 8] = y; }
      const v4f lc = *(const LAS v4f*)(L + L_LAM + ((ST0 + i) * 16 + c) * 16);
      const C2 l1 = {lc.x, lc.y}, l2 = csq(l1), l4 = csq(l2), l8 = csq(l4), l16 = csq(l8);
      C2 eE[2], eO[2], U[2];
#pragma unroll
      for (int sg = 0; sg < 2; ++sg) {
        C2 e = {a[4 * sg], a[4 * sg + 8]};
#pragma unroll
        for (int k = 1; k < 4; ++k) e = cfma(l1, e, (C2){a[4 * sg + k], a[4 * sg + k + 8]});
        eE[sg] = e; eO[sg] = e;
        swap32(eE[sg].r, eO[sg].r); swap32(eE[sg].i, eO[sg].i);
        U[sg] = cfma(l4, eE[sg], eO[sg]);
      }
      C2 V0 = cfma(l8, U[0], U[1]), V1 = V0;
      swap16(V0.r, V1.r); swap16(V0.i, V1.i);
      Eown[i] = cfma(l16, V0, V1);
      C2 c8[2];
      c8[0] = rho ? V0 : (C2){0.f, 0.f};
      c8[1] = cfma(l8, c8[0], U[0]);
#pragma unroll
      for (int sg = 0; sg < 2; ++sg) {
        C2 t4 = cfma(l4, c8[sg], eE[sg]);
        C2 c4 = h ? t4 : c8[sg];
        C2 u0 = cfma(l1, c4, (C2){a[4 * sg], a[4 * sg + 8]});
        a[4 * sg] = u0.r; a[4 * sg + 8] = u0.i;
      }
      if (tg < 3 && lane < 16) *(LAS v2f*)(L + L_EX + (((ST0 + i) * 3 + tg) * 16 + c) * 8) = (v2f){Eown[i].r, Eown[i].i};
      asm volatile("" : "+v"(a), "+v"(Eown[i].r), "+v"(Eown[i].i));
    }
    if (tg < 3) {
      asm volatile("s_waitcnt lgkmcnt(0)" ::: "memory");
      lds_st(L_EF + (grp * 4 + tg) * 4, 1u);
    } else if (m1 < 15) {
      W.four(L_EF + grp * 16, 3);
#pragma unroll
      for (int i = 0; i < NS; ++i) {
        const v4f lc = *(const LAS v4f*)(L + L_LAM + ((ST0 + i) * 16 + c) * 16);
        const C2 l32 = csq(csq(csq(csq(csq((C2){lc.x, lc.y})))));
        const v2f e0 = *(const LAS v2f*)(L + L_EX + (((ST0 + i) * 3 + 0) * 16 + c) * 8);
        const v2f e1 = *(const LAS v2f*)(L + L_EX + (((ST0 + i) * 3 + 1) * 16 + c) * 8);
        const v2f e2 = *(const LAS v2f*)(L + L_EX + (((ST0 + i) * 3 + 2) * 16 + c) * 8);
        C2 tot = cfma(l32, (C2){e0.x, e0.y}, (C2){e1.x, e1.y});
        tot = cfma(l32, tot, (C2){e2.x, e2.y});
        tot = cfma(l32, tot, Eown[i]);
        if (lane < 16) {
          GAS unsigned long long* gp = (GAS unsigned long long*)(ws + WS_GRAN + ((m1 * 32 + sig0 + ST0 + i) * 16 + c) * 16);
          __hip_atomic_store(gp, ((unsigned long long)tag << 32) | __float_as_uint(tot.r), __ATOMIC_RELAXED, __HIP_MEMORY_SCOPE_AGENT);
          __hip_atomic_store(gp + 1, ((unsigned long long)tag << 32) | __float_as_uint(tot.i), __ATOMIC_RELAXED, __HIP_MEMORY_SCOPE_AGENT);
        }
      }
    }
  }
  if constexpr (NF > 0) {
#pragma unroll
    for (int i = 0; i < NF; ++i) {
      float o[8];
#pragma unroll
      for (int r = 0; r < 8; ++r) {
        const float g = accF[i][r], u = accF[i][r + 8];
        const float e = __builtin_amdgcn_exp2f(g * -1.44269504088896f);
        o[r] = g * __builtin_amdgcn_rcpf(1.f + e) * u;
      }
      v4u v = {pk_bf16(o[0], o[1]), pk_bf16(o[2], o[3]), pk_bf16(o[4], o[5]), pk_bf16(o[6], o[7])};
      __builtin_amdgcn_raw_buffer_store_b128(v, wsr, WS_AH + (tgG * 176 + j1 * 8 + FT0 + i) * 1024 + lane * 16, 0, 16);
    }
    asm volatile("s_waitcnt vmcnt(0)" ::: "memory");
    unsigned old = 0;
    if (lane == 0) old = lds_add_rtn(L_CNT1, 1u);
    if (rfl(old) == 7u && lane == 0) __hip_atomic_store((GAS unsigned*)(ws + WS_FLAG + (m1 * 16 + j1) * 128), tag, __ATOMIC_RELAXED, __HIP_MEMORY_SCOPE_AGENT);
    if (wave == 0) ST(3);
  }
  if constexpr (NS > 0) {
    const int lg = lane >> 4;
#pragma unroll
    for (int i = 0; i < NS; ++i) {
      if ((i & 3) == tg) {
        C2 cin = {0.f, 0.f};
        if (m1 > 0) {
          const v4f lc = *(const LAS v4f*)(L + L_LAM + ((ST0 + i) * 16 + c) * 16);
          const C2 l128 = csq(csq(csq(csq(csq(csq(csq((C2){lc.x, lc.y}))))))), l256 = csq(l128), l512 = csq(l256), l384 = cmul(l128, l256);
          v4u gv[4];
#pragma nounroll
          for (;;) {
            asm volatile("" ::: "memory");
            bool ok = true;
#pragma unroll
            for (int k = 0; k < 4; ++k) {
              int mp = m1 - 1 - (4 * k + lg);
              int mc = mp < 0 ? 0 : mp;
              gv[k] = __builtin_amdgcn_raw_buffer_load_b128(wsr, WS_GRAN + ((mc * 32 + sig0 + ST0 + i) * 16 + c) * 16, 0, 16);
            }
#pragma unroll
            for (int k = 0; k < 4; ++k) ok = ok && gv[k].y == tag && gv[k].w == tag;
            if (__all(ok) || !W.spin()) break;
          }
          C2 acc = {0.f, 0.f};
#pragma unroll
          for (int k = 3; k >= 0; --k) {
            const bool valid = (m1 - 1 - (4 * k + lg)) >= 0;
            C2 e = {valid ? __uint_as_float(gv[k].x) : 0.f, valid ? __uint_as_float(gv[k].z) : 0.f};
            acc = cfma(l512, acc, e);
          }
          C2 wgt = lg == 0 ? (C2){1.f, 0.f} : lg == 1 ? l128 : lg == 2 ? l256 : l384;
          acc = cmul(acc, wgt);
          C2 o = acc; swap16(acc.r, o.r); swap16(acc.i, o.i); acc.r += o.r; acc.i += o.i;
          o = acc; swap32(acc.r, o.r); swap32(acc.i, o.i); acc.r += o.r; acc.i += o.i;
          cin = acc;
        }
        if (lane < 16) *(LAS v2f*)(L + L_CIN + ((ST0 + i) * 16 + c) * 8) = (v2f){cin.r, cin.i};
      }
    }
    asm volatile("s_waitcnt lgkmcnt(0)" ::: "memory");
    lds_st(L_CF + (grp * 4 + tg) * 4, 1u);
    W.four(L_CF + grp * 16, NS < 4 ? NS : 4);
    W.four(L_EF + grp * 16, tg);
#pragma unroll
    for (int i = 0; i < NS; ++i) {
      f32x16& a = accS[i];
      const v4f lc = *(const LAS v4f*)(L + L_LAM + ((ST0 + i) * 16 + c) * 16);
      const C2 l1 = {lc.x, lc.y}, l2 = csq(l1), l4 = csq(l2), l8 = csq(l4), l16 = csq(l8), l32 = csq(l16);
      const v2f ci = *(const LAS v2f*)(L + L_CIN + ((ST0 + i) * 16 + c) * 8);
      C2 P = {ci.x, ci.y};
      for (int w = 0; w < tg; ++w) {
        const v2f e = *(const LAS v2f*)(L + L_EX + (((ST0 + i) * 3 + w) * 16 + c) * 8);
        P = cfma(l32, P, (C2){e.x, e.y});
      }
      C2 t = cmul(l4, P); P = h ? t : P;
      t = cmul(l16, P); P = rho ? t : P;
      C2 s[2] = {P, cmul(l8, P)};
      unsigned pr[2][2], pi[2][2];
#pragma unroll
      for (int sg = 0; sg < 2; ++sg) {
        float orr[4], oi[4];
        C2 sv = s[sg];
#pragma unroll
        for (int k = 0; k < 4; ++k) {
          sv = cfma(l1, sv, (C2){a[4 * sg + k], a[4 * sg + k + 8]});
          orr[k] = lc.z * sv.r - lc.w * sv.i;
          oi[k] = -(lc.z * sv.i + lc.w * sv.r);
        }
        pr[sg][0] = pk_bf16(orr[0], orr[1]); pr[sg][1] = pk_bf16(orr[2], orr[3]);
        pi[sg][0] = pk_bf16(oi[0], oi[1]); pi[sg][1] = pk_bf16(oi[2], oi[3]);
      }
      swap32u(pr[0][0], pr[1][0]); swap32u(pr[0][1], pr[1][1]);
      swap32u(pi[0][0], pi[1][0]); swap32u(pi[0][1], pi[1][1]);
      const unsigned off = WS_AH + (tgG * 176 + 112 + sig0 + ST0 + i) * 1024 + c * 64 + rho * 32 + h * 16;
      __builtin_amdgcn_raw_buffer_store_b128((v4u){pr[0][0], pr[0][1], pr[1][0], pr[1][1]}, wsr, off, 0, 16);
      __builtin_amdgcn_raw_buffer_store_b128((v4u){pi[0][0], pi[0][1], pi[1][0], pi[1][1]}, wsr, off + 32 * 1024, 0, 16);
    }
    asm volatile("s_waitcnt vmcnt(0)" ::: "memory");
    unsigned old = 0;
    if (lane == 0) old = lds_add_rtn(L_CNT2, 1u);
    if (rfl(old) == (SSMBLK ? 7u : 3u) && lane == 0) __hip_atomic_store((GAS unsigned*)(ws + WS_FLAG + (m1 * 16 + j1) * 128 + 4), tag, __ATOMIC_RELAXED, __HIP_MEMORY_SCOPE_AGENT);
    if (wave == 4) ST(4);
  }
}

DI bool poll_flags(GAS char* ws, int m, int word, int n, unsigned tag, int lane, Wait& W) {
  const int j = lane < n ? lane : 0;
  GAS unsigned* p = (GAS unsigned*)(ws + WS_FLAG + (m * 16 + j) * 128 + word * 4);
#pragma nounroll
  for (;;) {
    unsigned v = __hip_atomic_load(p, __ATOMIC_RELAXED, __HIP_MEMORY_SCOPE_AGENT);
    if (__all(v == tag)) return true;
    if (!W.spin()) return false;
  }
}

extern "C" __global__ void __launch_bounds__(768, 3)
fused_kernel(const u16* __restrict__ x, const u16* __restrict__ Lre, const u16* __restrict__ Lim, const u16* __restrict__ Bre,
             const u16* __restrict__ Bim, const u16* __restrict__ Cre, const u16* __restrict__ Cim, const u16* __restrict__ ldt,
             const u16* __restrict__ Dsk, const u16* __restrict__ wg, const u16* __restrict__ wu, const u16* __restrict__ wd,
             const u16* __restrict__ gw, float* __restrict__ out, char* wsp) {
  extern __shared__ __attribute__((aligned(16))) char smem[];
  LAS char* L = (LAS char*)smem;
  GAS char* ws = (GAS char*)wsp;
  const int tid = threadIdx.x, lane = tid & 63, wave = rfl(tid >> 6);
  const int bid = blockIdx.x, xcd = bid & 7, idx = bid >> 3;
  const int m1 = (xcd & 1) * 8 + (idx & 7), j1 = (xcd >> 1) * 4 + (idx >> 3);
  const int rt = xcd * 4 + (idx >> 3), ct = idx & 7;
  const bool ssmblk = j1 >= 14;
  Wait W; W.budget = SPIN_CAP;
  if (wave == 0) ST(0);

  if (wave >= 8) {
    const unsigned lw = wave - 8;
    TS(ta); TS(tb); TS(tc); TS(td); TS(te); TS(tf); TS(tg_);
    const unsigned vr0 = (lane >> 3) * 1024 + (((lane & 7) ^ ((lane >> 4) & 7)) << 4);
    const unsigned vr1 = vr0 ^ 64;
    const unsigned fullw = L_FULL + lw * 4;
    {
      const unsigned c16 = lane & 15, q4 = lane >> 4;
      const unsigned vg = q4 * 3584 + (((c16 >> 1) ^ (q4 << 1)) << 5) + (c16 & 1) * 16;
      const unsigned vu = q4 * 3584 + (((c16 >> 1) ^ ((q4 << 1) | 1)) << 5) + (c16 & 1) * 16;
      const GAS char* xs = (const GAS char*)x + (size_t)(m1 * 128 + lw * 32) * 1024;
      const GAS char* gs = (const GAS char*)wg + (size_t)lw * 16 * 3584 + j1 * 256;
      const GAS char* us = (const GAS char*)wu + (size_t)lw * 16 * 3584 + j1 * 256;
      const int sig0 = ssmblk ? (14 + (j1 - 14) * 9) : j1;
      const GAS char* bs = (const GAS char*)(lw < 2 ? Bre : Bim) + (size_t)(sig0 * 16 + (lw & 1) * 8) * 1024;
      const unsigned vb = (lw & 1) ? vr1 : vr0;
      unsigned kk = ROT1 ? ((idx & 7) + 2 * (idx >> 3)) & 7 : 0;
      unsigned sbase = SLOT1;
#pragma unroll 1
      for (unsigned t = 0; t < 8; ++t) {
        if (t >= 3) W.min8w(L_PROG, t - 2);
        if (t == 0) TSET(tb);
        if (t == 1) TSET(tc);
        if (t == 4) TSET(tf);
        const GAS char* xp = xs + kk * 128;
        LAS char* lp = L + sbase + lw * 4096;
        GLDS(xp + vr0, lp, 0);
        GLDS(xp + 8192 + vr1, lp + 1024, 0);
        GLDS(xp + 16384 + vr0, lp + 2048, 0);
        GLDS(xp + 24576 + vr1, lp + 3072, 0);
        if (!ssmblk) {
          const GAS char* gp = gs + kk * (64 * 3584);
          const GAS char* up = us + kk * (64 * 3584);
#pragma unroll
          for (int p = 0; p < 4; ++p) {
            GLDS(gp + p * 4 * 3584 + vg, lp + 16384 + p * 1024, 0);
            GLDS(up + p * 4 * 3584 + vu, lp + 32768 + p * 1024, 0);
          }
          GLDS(bs + kk * 128 + vb, L + sbase + 49152 + lw * 1024, 0);
        } else {
#pragma unroll
          for (int p = 0; p < 9; ++p) GLDS(bs + p * 16384 + kk * 128 + vb, L + sbase + 16384 + p * 4096 + lw * 1024, 0);
        }
        kk = (kk + 1) & 7;
        sbase += SLOT1; if (sbase == 3 * SLOT1) sbase = 0;
        if (t == 2) {
          TSET(td);
          __builtin_amdgcn_s_barrier();
          TSET(te);
          WAIT_VM(26); lds_st(fullw, 1u);
          if (lw == 0) ST(1);
        }
        if (t == 4) TSET(tg_);
        if (t >= 2) { WAIT_VM(13); lds_st(fullw, t); }
      }
    }
    const unsigned vd = (lane >> 3) * 1024 + (((lane & 7) ^ (((lane >> 4) & 1) << 2)) << 4);
    const unsigned vl = lane * 16;
    const unsigned rotF = ROT2 ? (rt & 3) * 7 : 0, rotS = ROT2 ? (rt & 3) * 4 : 0;
    const GAS char* ah = ws + WS_AH + (size_t)(rt * 2 + (lw >> 1)) * 176 * 1024 + (lw & 1) * 2048;
    const GAS char* wds = (const GAS char*)wd + (size_t)lw * 16 * 1024 + ct * 128;
    const GAS char* crs = (const GAS char*)Cre + (size_t)(ct * 64 + lw * 16) * 1024;
    const GAS char* cis = (const GAS char*)Cim + (size_t)(ct * 64 + lw * 16) * 1024;
    unsigned ub = rotF, ua = rotF;
    unsigned db = lw * 2048, da = A2OFF + lw * 2048;
    unsigned prog = 0, npub = 8;
    auto issBF = [&]() {
      const GAS char* sp_ = wds + ub * 65536;
      GLDS(sp_ + vd, L + db, 0); GLDS(sp_ + 8192 + vd, L + db + 1024, 0);
      ub = ub == 27 ? 0 : ub + 1;
      db = db >= (RB2 - 1) * 8192 ? db - (RB2 - 1) * 8192 : db + 8192;
    };
    auto issBS = [&]() {
      const GAS char* sp_ = (ub & 8 ? cis : crs) + (ub & 7) * 128;
      GLDS(sp_ + vr0, L + db, 0); GLDS(sp_ + 8192 + vr1, L + db + 1024, 0);
      ub = (ub + 1) & 15;
      db = db >= (RB2 - 1) * 8192 ? db - (RB2 - 1) * 8192 : db + 8192;
    };
    auto issA = [&](unsigned last, unsigned first) {
      const GAS char* sp_ = ah + ua * 4096;
      GLDS(sp_ + vl, L + da, 16); GLDS(sp_ + 1024 + vl, L + da + 1024, 16);
      ua = ua == last ? first : ua + 1;
      da = da >= A2OFF + (RA2 - 1) * 8192 ? da - (RA2 - 1) * 8192 : da + 8192;
    };
    W.min8w(L_PROG, 6);
#pragma unroll 1
    for (int i = 0; i < 6; ++i) issBF();
    WAIT_VM(12); lds_st(fullw, 8u);
    if (lw == 0) ST(8);
    W.min8w(L_PROG, 7);
#pragma unroll 1
    for (int i = 0; i < 7; ++i) issBF();
    W.min8w(L_PROG, 8);
    if (lw == 0) {
      W.word(L_INIT, 8);
      const unsigned tag = rfl(lds_ld1(L_TAG));
      poll_flags(ws, rt >> 1, 0, 14, tag, lane, W);
      lds_st(L_RDY1, 1u);
      ST(5);
    } else W.word(L_RDY1, 1);
#pragma unroll 1
    for (int i = 0; i < 6; ++i) issA(27, 0);
    WAIT_VM(10); lds_st(fullw, 9u);
    WAIT_VM(8); lds_st(fullw, 10u);
    WAIT_VM(6); lds_st(fullw, 11u);
    npub = 11;
#define STEP(BISS, ALAST, AFIRST, NV) do { if (prog < npub - 2) prog = W.min4w(L_PROG, npub - 2); BISS; issA(ALAST, AFIRST); WAIT_VM(NV); ++npub; lds_st(fullw, npub); } while (0)
    STEP(issBF(), 27, 0, 8);
    STEP(issBF(), 27, 0, 10);
#pragma unroll 1
    for (int u = 2; u < 15; ++u) STEP(issBF(), 27, 0, 12);
    ub = rotS;
#pragma unroll 1
    for (int u = 15; u < 22; ++u) STEP(issBS(), 27, 0, 12);
    if (rfl(lds_ld1(L_RDY2)) == 0) {
      WAIT_VM(0); lds_st(fullw, npub + 3);
      W.word(L_RDY2, 1);
      ua = 28 + rotS;
      for (int i = 0; i < 3; ++i) { if (prog < npub - 2) prog = W.min4w(L_PROG, npub - 2); issBS(); issA(43, 28); ++npub; }
    } else {
      ua = 28 + rotS;
      for (int i = 0; i < 3; ++i) STEP(issBS(), 43, 28, 12);
    }
#pragma unroll 1
    for (int u = 25; u < 31; ++u) STEP(issBS(), 43, 28, 12);
#pragma unroll 1
    for (int u = 31; u < 38; ++u) STEP((void)0, 43, 28, 6);
    WAIT_VM(4); lds_st(fullw, 50u);
    WAIT_VM(2); lds_st(fullw, 51u);
    WAIT_VM(0); lds_st(fullw, 52u);
    if (lw == 0) { TW(9, ta); TW(10, tb); TW(11, tc); TW(12, td); TW(13, te); TW(14, tf); TW(15, tg_); }
    return;
  }

  unsigned cpf;
  {
    unsigned long long pc; unsigned len, here;
    asm volatile("s_getpc_b64 %0\n.Lpf_here:\n\ts_mov_b32 %1, 0xc700\n\ts_mov_b32 %2, .Lpf_here-fused_kernel" : "=s"(pc), "=s"(len), "=s"(here));
    unsigned off = tid * 128; off = off < len - 4 ? off : 0;
    cpf = *(const GAS unsigned*)(pc - here + off);
  }
  unsigned tpf = 0;
  if (wave == 7) {
    const GAS char* p;
    if (lane < 26) p = ws + lane * 524288;
    else if (lane < 34) p = (const GAS char*)out + (lane - 26) * 524288;
    else if (lane < 41) p = (const GAS char*)wd + (lane - 34) * 262144;
    else if (lane < 45) p = (const GAS char*)Cre + (lane - 41) * 131072;
    else if (lane < 49) p = (const GAS char*)Cim + (lane - 45) * 131072;
    else p = (const GAS char*)Dsk;
    tpf = *(const GAS unsigned*)p;
  }
  if (tid < 64) *(LAS unsigned*)(L + MISC + tid * 4) = 0u;
  asm volatile("s_waitcnt lgkmcnt(0)" ::: "memory");
  __builtin_amdgcn_s_barrier();
  asm volatile("" ::: "memory");
  const __amdgpu_buffer_rsrc_t wsr = __builtin_amdgcn_make_buffer_rsrc((void*)wsp, 0, WS_END, 0x00020000);
  {
    const int tok = m1 * 128 + j1 * 8 + wave;
    const v4u xv = *(const GAS v4u*)((const GAS char*)x + (size_t)tok * 1024 + lane * 16);
    const v4u gv = *(const GAS v4u*)((const GAS char*)gw + lane * 16);
    if (tid == 0) *(LAS unsigned*)(L + L_TAG) = __hip_atomic_load((GAS unsigned*)(ws + WS_CTL), __ATOMIC_RELAXED, __HIP_MEMORY_SCOPE_AGENT) + 1u;
    const int nch = ssmblk ? 144 : 16;
    const int t2 = tid - 64;
    if (t2 >= 0 && t2 < nch) {
      const int p = (ssmblk ? (14 + (j1 - 14) * 9) : j1) * 16 + t2;
      const float lr = bf2f(Lre[p]), li = bf2f(Lim[p]);
      const float dt = __bfloat162float(__float2bfloat16(expf(bf2f(ldt[p]))));
      const float e = expf(dt * lr);
      float sn, cs; sincosf(dt * li, &sn, &cs);
      const float ar = e * cs, ai = e * sn;
      const float nr = ar - 1.f, den = 2.f / (lr * lr + li * li);
      *(LAS v4f*)(L + L_LAM + t2 * 16) = (v4f){ar, ai, (nr * lr + ai * li) * den, (ai * lr - nr * li) * den};
    }
    float s = 0.f;
#pragma unroll
    for (int i = 0; i < 4; ++i) { const unsigned a = xv[i], b = gv[i]; s = __builtin_amdgcn_fdot2_f32_bf16(__builtin_bit_cast(bf16x2, a), __builtin_bit_cast(bf16x2, b), s, false); }
#pragma unroll
    for (int o = 32; o >= 1; o >>= 1) s += __shfl_xor(s, o);
    const float g = 1.f / (1.f + __expf(-s));
    if (lane == 0) __hip_atomic_store((GAS unsigned*)(ws + WS_G + tok * 4), __float_as_uint(g), __ATOMIC_RELAXED, __HIP_MEMORY_SCOPE_AGENT);
    asm volatile("s_waitcnt lgkmcnt(0)" ::: "memory");
    asm volatile("" :: "v"(cpf), "v"(tpf));
    if (lane == 0) lds_add(L_INIT, 1u);
  }
  unsigned tag;
  if (!ssmblk) {
    if (wave < 4) phase1<5, 0, 0, 0, false>(L, ws, wsr, W, wave, lane, m1, j1, tag);
    else phase1<3, 5, 1, 0, false>(L, ws, wsr, W, wave, lane, m1, j1, tag);
  } else {
    if (wave < 4) phase1<0, 0, 4, 0, true>(L, ws, wsr, W, wave, lane, m1, j1, tag);
    else phase1<0, 0, 5, 4, true>(L, ws, wsr, W, wave, lane, m1, j1, tag);
  }
  if (wave >= 4) {
    if (wave == 4) {
      const bool ok = poll_flags(ws, rt >> 1, 1, 16, tag, lane, W);
      lds_st(L_RDY2, 1u);
      ST(6);
      if (ok && bid == 255 && lane == 0) __hip_atomic_store((GAS unsigned*)(ws + WS_CTL), tag, __ATOMIC_RELAXED, __HIP_MEMORY_SCOPE_AGENT);
    }
    return;
  }
  {
    const int tq = wave & 1, dq = wave >> 1;
    const int h = lane >> 5, r32 = lane & 31;
    const int g4 = lane >> 4, q = (lane & 15) >> 2, p = lane & 3;
    const unsigned aF = tq * 4096 + lane * 16;
    const unsigned bF = (4 * (g4 >> 1) + q) * 128 + ((dq ^ (q >> 1)) << 6) + (g4 & 1) * 32 + p * 8;
    const unsigned aS = tq * 4096 + (8 * (g4 >> 1) + q) * 64 + (g4 & 1) * 32 + p * 8;
    unsigned bS[4];
#pragma unroll
    for (int ks = 0; ks < 4; ++ks) bS[ks] = (dq * 32 + r32) * 128 + (((2 * ks + h) ^ ((r32 >> 1) & 7)) << 4);
    const int tok = rt * 64 + tq * 32 + r32, d0 = ct * 64 + dq * 32 + 4 * h;
    v2u xsk[4], dsk[4];
#pragma unroll
    for (int g = 0; g < 4; ++g) {
      xsk[g] = *(const GAS v2u*)((const GAS char*)x + (size_t)tok * 1024 + (d0 + 8 * g) * 2);
      dsk[g] = *(const GAS v2u*)((const GAS char*)Dsk + (d0 + 8 * g) * 2);
    }
    f32x16 accF = {}, accS = {};
    unsigned sa = A2OFF, sbb = 0, full = 0;
    const unsigned progw = L_PROG + wave * 4;
#pragma unroll 1
    for (unsigned n = 9; n < 9 + 28; ++n) {
      if (full < n) full = W.min4w(L_FULL, n);
      bf16x8 af[4], bf[4];
#pragma unroll
      for (int ks = 0; ks < 4; ++ks) { af[ks] = ld_b128(L, sa + aF + ks * 1024); bf[ks] = ld_tr2(L, sbb + bF + ks * 2048, 1024); }
      asm volatile("s_waitcnt lgkmcnt(0)" ::: "memory");
      lds_st(progw, n);
#pragma unroll
      for (int ks = 0; ks < 4; ++ks) accF = MFMA32(bf[ks], af[ks], accF);
      sa = sa == A2OFF + (RA2 - 1) * 8192 ? A2OFF : sa + 8192;
      sbb = sbb == (RB2 - 1) * 8192 ? 0 : sbb + 8192;
    }
#pragma unroll 1
    for (unsigned n = 9 + 28; n < 9 + 44; ++n) {
      if (full < n) full = W.min4w(L_FULL, n);
      bf16x8 af[4], bf[4];
#pragma unroll
      for (int ks = 0; ks < 4; ++ks) { af[ks] = ld_tr2(L, sa + aS + ks * 1024, 256); bf[ks] = ld_b128(L, sbb + bS[ks]); }
      asm volatile("s_waitcnt lgkmcnt(0)" ::: "memory");
      lds_st(progw, n);
#pragma unroll
      for (int ks = 0; ks < 4; ++ks) accS = MFMA32(bf[ks], af[ks], accS);
      sa = sa == A2OFF + (RA2 - 1) * 8192 ? A2OFF : sa + 8192;
      sbb = sbb == (RB2 - 1) * 8192 ? 0 : sbb + 8192;
    }
    const float g = __uint_as_float(__hip_atomic_load((GAS unsigned*)(ws + WS_G + tok * 4), __ATOMIC_RELAXED, __HIP_MEMORY_SCOPE_AGENT));
    const float g1 = 1.f - g;
#pragma unroll
    for (int gq = 0; gq < 4; ++gq) {
      v4f o;
#pragma unroll
      for (int i = 0; i < 4; ++i) {
        const unsigned xw = xsk[gq][i >> 1], dw = dsk[gq][i >> 1];
        const float xv = __uint_as_float((i & 1) ? (xw & 0xffff0000u) : (xw << 16));
        const float dv = __uint_as_float((i & 1) ? (dw & 0xffff0000u) : (dw << 16));
        o[i] = g * (accS[4 * gq + i] + xv * dv) + g1 * accF[4 * gq + i];
      }
      *(GAS v4f*)((GAS char*)out + ((size_t)tok * 512 + d0 + 8 * gq) * 4) = o;
    }
    if (wave == 0) ST(7);
  }
}

extern "C" void kernel_launch(void* const* d_in, const int* in_sizes, int n_in, void* d_out, int out_size, void* d_ws, size_t ws_size, hipStream_t stream) {
  (void)in_sizes; (void)n_in; (void)out_size; (void)ws_size;
  (void)hipFuncSetAttribute((const void*)fused_kernel, hipFuncAttributeMaxDynamicSharedMemorySize, LDS_BYTES);
  fused_kernel<<<256, 768, LDS_BYTES, stream>>>((const u16*)d_in[0], (const u16*)d_in[1], (const u16*)d_in[2], (const u16*)d_in[3], (const u16*)d_in[4],
                                                (const u16*)d_in[5], (const u16*)d_in[6], (const u16*)d_in[7], (const u16*)d_in[8], (const u16*)d_in[10],
                                                (const u16*)d_in[11], (const u16*)d_in[12], (const u16*)d_in[13], (float*)d_out, (char*)d_ws);
}
```

```cpp
#include <hip/hip_runtime.h>
#include <hip/hip_bf16.h>

#define GAS __attribute__((address_space(1)))
#define LAS __attribute__((address_space(3)))
#define DI __device__ __forceinline__
typedef unsigned short u16;
typedef unsigned long long u64;
typedef unsigned v4u __attribute__((ext_vector_type(4)));
typedef unsigned v2u __attribute__((ext_vector_type(2)));
typedef float v4f __attribute__((ext_vector_type(4)));
typedef float v2f __attribute__((ext_vector_type(2)));
typedef short s16x4 __attribute__((ext_vector_type(4)));
typedef __bf16 bf16x8 __attribute__((ext_vector_type(8)));
typedef float f32x16 __attribute__((ext_vector_type(16)));
typedef __bf16 bf16x2 __attribute__((ext_vector_type(2)));

#define ROT1 0

constexpr unsigned WS_CTL  = 0;
constexpr unsigned WS_FLAG = 4096;
constexpr unsigned WS_GRAN = WS_FLAG + 131072;
constexpr unsigned WS_G    = WS_GRAN + 131072;
constexpr unsigned WS_STAMP= 512u << 10;
constexpr unsigned WS_ACT  = 1u << 20;
constexpr unsigned WS_H    = WS_ACT + 16u * 56u * 8192u;
constexpr unsigned WS_PART = WS_H + 16u * 2u * 16u * 8192u;
constexpr unsigned WS_END  = WS_PART + 256u * 3u * 8192u;

constexpr unsigned SLOT1 = 53248;
constexpr unsigned RB2 = 10, RA2 = 8;
constexpr unsigned A2OFF = RB2 * 8192;
constexpr unsigned L_DUMP = A2OFF + RA2 * 8192;
constexpr unsigned MISC  = 3 * SLOT1;
constexpr unsigned L_FAB  = MISC + 0;
constexpr unsigned L_FP   = MISC + 256;
constexpr unsigned L_R    = MISC + 512;
constexpr unsigned L_OWN  = MISC + 540;
constexpr unsigned L_DMP  = MISC + 544;
constexpr unsigned L_EF   = MISC + 552;
constexpr unsigned L_CF   = MISC + 564;
constexpr unsigned L_CNT  = MISC + 572;
constexpr unsigned N_CTL_WORDS  = 148;
constexpr unsigned L_TAG  = MISC + 592;
constexpr unsigned L_E0   = MISC + 640;
constexpr unsigned L_LAM  = MISC + 1536;
constexpr unsigned LDS_BYTES = 163840;

constexpr unsigned SPIN_CAP = 1u << 20;

DI float bf2f(unsigned v) { return __uint_as_float(v << 16); }
DI unsigned pk_bf16(float a, float b) { v2f p = {a, b}; return __builtin_bit_cast(unsigned, __builtin_convertvector(p, bf16x2)); }
DI unsigned rfl(unsigned v) { return __builtin_amdgcn_readfirstlane(v); }

DI void lds_st(unsigned addr, unsigned v) {
  asm volatile("s_mov_b64 exec, 1\n\tds_write_b32 %0, %1\n\ts_mov_b64 exec, -1" ::"v"(addr), "v"(v) : "memory");
}
DI unsigned lds_ld1(unsigned addr) { unsigned r; asm volatile("ds_read_b32 %0, %1\n\ts_waitcnt lgkmcnt(0)" : "=&v"(r) : "v"(addr) : "memory"); return rfl(r); }
DI void dma_flag(unsigned lds_addr, u64 src) {
  unsigned keep;
  asm volatile("s_mov_b32 %0, m0\n\ts_mov_b32 m0, %1\n\ts_mov_b64 exec, 1\n\tglobal_load_lds_dword %2, %3\n\ts_mov_b64 exec, -1\n\ts_mov_b32 m0, %0"
               : "=&s"(keep) : "s"(lds_addr), "v"(0u), "s"(src) : "memory");
}
DI void lds_inc(unsigned addr) {
  u64 ex;
  asm volatile("s_mov_b64 %0, exec\n\ts_mov_b64 exec, 1\n\tds_add_u32 %1, %2\n\ts_mov_b64 exec, %0" : "=&s"(ex) : "v"(addr), "v"(1u) : "memory");
}
#define WAIT_VM(n) asm volatile("s_waitcnt vmcnt(%0)" ::"i"(n) : "memory")
#define WAIT_LGKM0() asm volatile("s_waitcnt lgkmcnt(0)" ::: "memory")

#define TRX(row, i) do {} while (0)
struct Wait {
  unsigned budget;
  DI void ge(unsigned vaddr, unsigned need) {
    unsigned t; budget = rfl(budget);
    asm volatile("1:\n\tds_read_b32 %0, %2\n\ts_waitcnt lgkmcnt(0)\n\tv_cmp_gt_u32 vcc, %3, %0\n\ts_cbranch_vccz 2f\n\ts_sleep 1\n\t"
                 "s_sub_u32 %1, %1, 1\n\ts_cbranch_scc0 1b\n\ts_mov_b32 %1, 0\n2:"
                 : "=&v"(t), "+s"(budget) : "v"(vaddr), "s"(need) : "memory", "vcc", "scc");
  }
  DI bool spin() { __builtin_amdgcn_s_sleep(1); if (budget == 0) return false; --budget; return true; }
};

struct C2 { float r, i; };
DI C2 cmul(C2 a, C2 b) { return {a.r * b.r - a.i * b.i, a.r * b.i + a.i * b.r}; }
DI C2 cfma(C2 a, C2 b, C2 c) { return {fmaf(a.r, b.r, fmaf(-a.i, b.i, c.r)), fmaf(a.r, b.i, fmaf(a.i, b.r, c.i))}; }
DI C2 csq(C2 a) { return {a.r * a.r - a.i * a.i, (a.r + a.r) * a.i}; }
DI C2 csel(bool p, C2 a, C2 b) { return {p ? a.r : b.r, p ? a.i : b.i}; }
DI void swap32(float& x, float& y) { auto r = __builtin_amdgcn_permlane32_swap(__float_as_uint(x), __float_as_uint(y), false, false); x = __uint_as_float(r[0]); y = __uint_as_float(r[1]); }
DI void swap16(float& x, float& y) { auto r = __builtin_amdgcn_permlane16_swap(__float_as_uint(x), __float_as_uint(y), false, false); x = __uint_as_float(r[0]); y = __uint_as_float(r[1]); }
DI void swap16u(unsigned& x, unsigned& y) { auto r = __builtin_amdgcn_permlane16_swap(x, y, false, false); x = r[0]; y = r[1]; }

#define MFMA(a, b, c) __builtin_amdgcn_mfma_f32_16x16x32_bf16((a), (b), (c), 0, 0, 0)
#define MFMA32(a, b, c) __builtin_amdgcn_mfma_f32_32x32x16_bf16((a), (b), (c), 0, 0, 0)
#define GLDS(g, l, aux) __builtin_amdgcn_global_load_lds((const GAS void*)(g), (LAS void*)(l), 16, 0, (aux))
#define GLDSO(g, l, off, aux) __builtin_amdgcn_global_load_lds((const GAS void*)(g), (LAS void*)(l), 16, (off), (aux))
#define SB() __builtin_amdgcn_sched_barrier(0)
#define RLX_AGENT __ATOMIC_RELAXED, __HIP_MEMORY_SCOPE_AGENT

struct LdsBase { DI LAS char* operator+(unsigned off) const { return (LAS char*)(size_t)off; } };
DI bf16x8 ld_b128(LdsBase L, unsigned off) { return *(const LAS bf16x8*)(L + off); }
DI bf16x8 ld_tr2(LdsBase L, unsigned off, unsigned d) {
  s16x4 a = __builtin_amdgcn_ds_read_tr16_b64_v4i16((LAS s16x4*)(L + off));
  s16x4 b = __builtin_amdgcn_ds_read_tr16_b64_v4i16((LAS s16x4*)(L + off + d));
  return __builtin_bit_cast(bf16x8, __builtin_shufflevector(a, b, 0, 1, 2, 3, 4, 5, 6, 7));
}

#define ST(i) do {} while (0)

DI void arrive(GAS char* myflag, int grp, unsigned last, unsigned tag, int lane) {
  unsigned r;
  asm volatile("s_mov_b64 exec, 1\n\tds_add_rtn_u32 %0, %1, %2\n\ts_waitcnt lgkmcnt(0)\n\ts_mov_b64 exec, -1" : "=&v"(r) : "v"(L_CNT + grp * 4), "v"(1u) : "memory");
  if (rfl(r) == last && lane == 0) __hip_atomic_store((GAS unsigned*)(myflag + grp * 2048), tag, RLX_AGENT);
}
#define BAR() do { asm volatile("" ::: "memory"); __builtin_amdgcn_s_barrier(); asm volatile("" ::: "memory"); } while (0)
#define WAIT_LGKM_C() __builtin_amdgcn_s_waitcnt(0xC07F)
#define WAIT_ALL_C() __builtin_amdgcn_s_waitcnt(0x0070)

#define PIN_ACC() do { _Pragma("unroll") for (int a_ = 0; a_ < 4; ++a_) asm volatile("" : "+v"(acc[a_][0]), "+v"(acc[a_][1]), "+v"(acc[a_][2]), "+v"(acc[a_][3])); asm volatile("" : "+v"(r9), "+v"(i9)); } while (0)
struct Pair { v4f r[4], i[4]; };

DI void ssm_part1(const Pair& P, LdsBase L, int pi, int lane, C2& cl, C2& E) {
  const v4f lc = *(const LAS v4f*)(L + L_LAM + (pi * 16 + (lane & 15)) * 32);
  const v2f lp = *(const LAS v2f*)(L + L_LAM + (pi * 16 + (lane & 15)) * 32 + 16);
  const C2 l1 = {lc.x, lc.y}, l16 = {lp.x, lp.y}, l32 = csq(l16);
  C2 e = {P.r[0][0], P.i[0][0]};
#pragma unroll
  for (int n = 1; n < 16; ++n) e = cfma(l1, e, (C2){P.r[n >> 2][n & 3], P.i[n >> 2][n & 3]});
  C2 eE = e, eO = e;
  swap16(eE.r, eO.r); swap16(eE.i, eO.i);
  C2 Ulo = cfma(l16, eE, eO), Uhi = Ulo;
  swap32(Ulo.r, Uhi.r); swap32(Ulo.i, Uhi.i);
  E = cfma(l32, Ulo, Uhi);
  const C2 c = csel(lane & 32, Ulo, (C2){0.f, 0.f});
  cl = csel(lane & 16, cfma(l16, c, eE), c);
}
DI void ssm_publish(LdsBase L, GAS char* ws, int pi, int lane, int m, int sig, unsigned tag, C2 E) {
  const int c = lane & 15;
  const v2f lp = *(const LAS v2f*)(L + L_LAM + (pi * 16 + c) * 32 + 16);
  const C2 l64 = csq(csq((C2){lp.x, lp.y}));
  const v2f e0 = *(const LAS v2f*)(L + L_E0 + (pi * 16 + c) * 8);
  const C2 tot = cfma(l64, (C2){e0.x, e0.y}, E);
  if (lane < 32) {
    GAS u64* gp = (GAS u64*)(ws + WS_GRAN + ((m * 32 + sig) * 16 + c) * 16 + (lane >> 4) * 8);
    __hip_atomic_store(gp, ((u64)tag << 32) | __float_as_uint(lane & 16 ? tot.i : tot.r), RLX_AGENT);
  }
}
DI void ssm_reduce(LdsBase L, int pi, int lane, int m, const v4u (&gv)[4]) {
  const int c = lane & 15, q = lane >> 4;
  const v2f lp = *(const LAS v2f*)(L + (L_LAM + (pi * 16 + c) * 32 + 24));
  const C2 l128 = {lp.x, lp.y}, l256 = csq(l128), l512 = csq(l256);
  C2 acc = {0.f, 0.f};
#pragma unroll
  for (int k = 3; k >= 0; --k) {
    const bool valid = (m - 1 - (4 * k + q)) >= 0;
    acc = cfma(l512, acc, (C2){valid ? __uint_as_float(gv[k].x) : 0.f, valid ? __uint_as_float(gv[k].z) : 0.f});
  }
  acc = csel(lane & 16, cmul(acc, l128), acc);
  acc = csel(lane & 32, cmul(acc, l256), acc);
  C2 o = acc; swap16(acc.r, o.r); swap16(acc.i, o.i); acc.r += o.r; acc.i += o.i;
  o = acc; swap32(acc.r, o.r); swap32(acc.i, o.i); acc.r += o.r; acc.i += o.i;
  if (lane < 16) *(LAS v2f*)(L + (L_LAM + (pi * 16 + c) * 32 + 24)) = (v2f){acc.r, acc.i};
}
template <int NP>
DI void lookback(LdsBase L, __amdgpu_buffer_rsrc_t wsr, Wait& W, int lane, int m, int sig, int pi0, unsigned tag) {
  v4u gv[NP][4] = {};
  if (m > 0) {
    unsigned off[4];
#pragma unroll
    for (int k = 0; k < 4; ++k) { const int mp = m - 1 - (4 * k + (lane >> 4)); off[k] = WS_GRAN + (((mp < 0 ? 0 : mp) * 32 + sig) * 16 + (lane & 15)) * 16; }
#pragma nounroll
    for (;;) {
      asm volatile("" ::: "memory");
      bool ok = true;
#pragma unroll
      for (int p = 0; p < NP; ++p)
#pragma unroll
        for (int k = 0; k < 4; ++k) gv[p][k] = __builtin_amdgcn_raw_buffer_load_b128(wsr, off[k] + p * 256, 0, 16);
#pragma unroll
      for (int p = 0; p < NP; ++p)
#pragma unroll
        for (int k = 0; k < 4; ++k) ok = ok && gv[p][k].y == tag && gv[p][k].w == tag;
      if (__all(ok) || !W.spin()) break;
    }
  }
#pragma unroll
  for (int p = 0; p < NP; ++p) ssm_reduce(L, pi0 + p, lane, m, gv[p]);
}
DI C2 ssm_carry(LdsBase L, int pi, int lane, int th, C2 cl) {
  const int c = lane & 15;
  const v4f lp = *(const LAS v4f*)(L + (L_LAM + (pi * 16 + c) * 32 + 16));
  const C2 l16 = {lp.x, lp.y}, l32 = csq(l16);
  C2 acc = {lp.z, lp.w};
  if (th) { const v2f e0 = *(const LAS v2f*)(L + (L_E0 + (pi * 16 + c) * 8)); acc = cfma(csq(l32), acc, (C2){e0.x, e0.y}); }
  acc = csel(lane & 16, cmul(acc, l16), acc);
  acc = csel(lane & 32, cmul(acc, l32), acc);
  return {acc.r + cl.r, acc.i + cl.i};
}
DI void ssm_part2(const Pair& P, LdsBase L, __amdgpu_buffer_rsrc_t wsr, int pi, int lane, int m, int th, int sig, C2 s) {
  const int c = lane & 15, q = lane >> 4;
  const v4f lc = *(const LAS v4f*)(L + L_LAM + (pi * 16 + c) * 32);
  const C2 l1 = {lc.x, lc.y};
  const int pc = (c & 3) | ((c >> 1) & 4) | ((c << 1) & 8);
  const unsigned off = WS_H + ((m * 2 + th) * 16 + (q & 1) * 8 + (sig >> 2)) * 8192 + ((sig >> 1) & 1) * 1024 + (sig & 1) * 512 + pc * 32 + (q >> 1) * 16;
#pragma unroll
  for (int tt = 0; tt < 4; ++tt) {
    float orr[4], oi[4];
#pragma unroll
    for (int k = 0; k < 4; ++k) {
      s = cfma(l1, s, (C2){P.r[tt][k], P.i[tt][k]});
      orr[k] = lc.z * s.r - lc.w * s.i;
      oi[k] = -(lc.z * s.i + lc.w * s.r);
    }
    unsigned a0 = pk_bf16(orr[0], orr[1]), a1 = pk_bf16(orr[2], orr[3]), b0 = pk_bf16(oi[0], oi[1]), b1 = pk_bf16(oi[2], oi[3]);
    swap16u(a0, b0); swap16u(a1, b1);
    __builtin_amdgcn_raw_buffer_store_b128((v4u){a0, a1, b0, b1}, wsr, (off ^ ((tt & 1) * 128)) + tt * 2048, 0, 16);
  }
}

extern "C" __global__ void __launch_bounds__(768, 3)
fused_kernel(const u16* __restrict__ x, const u16* __restrict__ Lre, const u16* __restrict__ Lim, const u16* __restrict__ Bre,
             const u16* __restrict__ Bim, const u16* __restrict__ Cre, const u16* __restrict__ Cim, const u16* __restrict__ ldt,
             const u16* __restrict__ Dsk, const u16* __restrict__ wg, const u16* __restrict__ wu, const u16* __restrict__ wd,
             const u16* __restrict__ gw, float* __restrict__ out, char* wsp) {
  const LdsBase L;
  GAS char* ws = (GAS char*)wsp;
  const int tid = threadIdx.x, lane = tid & 63, wave = rfl(tid >> 6);
  const int bid = blockIdx.x, xcd = bid & 7, idx = bid >> 3;
  const int m = (xcd & 1) * 8 + (idx & 7), kap = xcd >> 1, cp = idx >> 3;
  const bool mixed = cp == 3;
  const unsigned nown = mixed ? 2 : 4;
  Wait W; W.budget = SPIN_CAP;
  u64 nz;
  asm volatile("s_getpc_b64 %0" : "=s"(nz));
  const __amdgpu_buffer_rsrc_t wsr = __builtin_amdgcn_make_buffer_rsrc((void*)wsp, 0, WS_END, 0x00020000);
  if (wave == 0) ST(0);

  if (wave >= 8) {
    const unsigned lw = wave - 8;
    __builtin_amdgcn_s_setprio(3);
    const unsigned r8 = lane >> 3, c16 = lane & 15, q4 = lane >> 4;
    const unsigned sw0 = ((lane & 7) ^ q4) << 4;
    const unsigned vr0 = r8 * 1024 + sw0, vr1 = vr0 ^ 64;
    {
      const unsigned vx0 = (16 * (r8 >> 2) + (r8 & 3)) * 1024 + sw0, vx1 = vx0 ^ 64;
      const GAS char* xs = (const GAS char*)x + (size_t)(m * 128 + (lw >> 1) * 64 + (lw & 1) * 8) * 1024;
      const unsigned j0 = (c16 >> 1) ^ q4;
      const unsigned wo = (lw * 16 + q4) * 3584 + kap * 896 + cp * 256 + (j0 & 3) * 32 + (c16 & 1) * 16;
      const GAS char* w0 = (const GAS char*)(j0 & 4 ? wu : wg) + wo;
      const GAS char* w1 = (const GAS char*)(j0 & 4 ? wg : wu) + wo + 8 * 3584;
      const GAS char* bs = (const GAS char*)(lw < 2 ? Bre : Bim) + (size_t)(kap * 128 + (lw & 1) * 8) * 1024;
      const unsigned vb = (lw & 1) ? vr1 : vr0;
      unsigned kk = ROT1 ? ((idx & 7) + 2 * cp) & 7 : 0;
      unsigned sbase = SLOT1;
#pragma unroll 1
      for (unsigned t = 0; t < 8; ++t) {
        const GAS char* xp = xs + kk * 128;
        LAS char* lp = L + sbase + lw * 4096;
        const unsigned ko = kk * (64 * 3584);
        GLDS(xp + vx0, lp, 0);
        GLDS(xp + 32 * 1024 + vx1, lp + 1024, 0);
        GLDS(xp + 4 * 1024 + vx0, lp + 2048, 0);
        GLDS(xp + 36 * 1024 + vx1, lp + 3072, 0);
        GLDS(w0 + ko, lp + 16384, 0);
        GLDS(w0 + ko + 4 * 3584, lp + 16384 + 1024, 0);
        GLDS(w1 + ko, lp + 16384 + 2048, 0);
        GLDS(w1 + ko + 4 * 3584, lp + 16384 + 3072, 0);
        GLDS(bs + cp * 16384 + kk * 128 + vb, L + sbase + 49152 + lw * 1024, 0);
        if (!mixed) {
          GLDS(w0 + ko + 128, lp + 32768, 0);
          GLDS(w0 + ko + 128 + 4 * 3584, lp + 32768 + 1024, 0);
          GLDS(w1 + ko + 128, lp + 32768 + 2048, 0);
          GLDS(w1 + ko + 128 + 4 * 3584, lp + 32768 + 3072, 0);
        } else {
#pragma unroll
          for (int p = 0; p < 4; ++p) GLDS(bs + (4 + p) * 16384 + kk * 128 + vb, L + sbase + 32768 + p * 4096 + lw * 1024, 0);
        }
        kk = (kk + 1) & 7;
        sbase += SLOT1; if (sbase == 3 * SLOT1) sbase = 0;
        if (t > 0) { WAIT_VM(13); if (lw == 0 && t == 1) ST(1); BAR(); }
      }
    }
    const unsigned par = lw & 1;
    const unsigned rotS = cp * 2 + (m & 1);
    if (lw >= 2) {
      unsigned vo[8];
      {
        const unsigned v0 = q4 * 1024 + (((c16 >> 1) ^ (q4 << 1)) << 5) + (c16 & 1) * 16;
#pragma unroll
        for (int p = 0; p < 8; ++p) vo[p] = ((p & 1) ? (v0 ^ 32) : v0) + p * 4096 - (p & 3) * 1024;
      }
      const GAS char* bbase = (const GAS char*)wd + (size_t)kap * 448 * 1024 + cp * 256;
#define B_ITEM(sp_, db, i) do { \
        GLDSO(sp_ + vo[0], L + db, 0, 0); GLDSO(sp_ + vo[1], L + db, 1024, 0); GLDSO(sp_ + vo[2], L + db, 2048, 0); GLDSO(sp_ + vo[3], L + db, 3072, 0); \
        GLDSO(sp_ + vo[4], L + db + 4096, 0, 0); GLDSO(sp_ + vo[5], L + db + 4096, 1024, 0); GLDSO(sp_ + vo[6], L + db + 4096, 2048, 0); GLDSO(sp_ + vo[7], L + db + 4096, 3072, 0); \
        dma_flag(L_FAB + (i) * 8 + 4, nz); } while (0)
      unsigned i = par;
#pragma unroll 1
      for (; i < 10; i += 2) {
        unsigned kb = 4 * cp + i; kb = kb >= 14 ? kb - 14 : kb;
        const GAS char* sp_ = bbase + kb * 32768;
        B_ITEM(sp_, i * 8192, i);
        if ((i >> 1) == 2) { WAIT_VM(27); BAR(); }
      }
      BAR();
      if (par == 0) {
        {
          const unsigned tag = lds_ld1(L_TAG);
          lookback<1>(L, wsr, W, lane, m, kap * 8 + cp, 4, tag);
          lds_st(L_CF, 1u);
          ST(15);
          if (mixed) { lookback<4>(L, wsr, W, lane, m, kap * 8 + 4, 0, tag); lds_st(L_CF + 4, 1u); }
        }
        const unsigned vfp = L_FP + (lane & 3) * 4;
        unsigned db = 0;
#pragma unroll 1
        for (i = 10; i < 14; ++i) {
          unsigned kb = 4 * cp + i; kb = kb >= 14 ? kb - 14 : kb;
          const GAS char* sp_ = bbase + kb * 32768;
          W.ge(vfp + ((i - 10) >> 1) * 16, 1);
          TRX(2, i);
          B_ITEM(sp_, db, i);
          db += 8192;
        }
#pragma unroll
        for (int p = 0; p < 8; ++p) vo[p] = ((p & 1) ? vr1 : vr0) + p * 8192 - (p & 3) * 1024;
        const unsigned co = (cp * 128 + (kap >> 1) * 64) * 1024;
#pragma unroll 1
        for (; i < 30; ++i) {
          const unsigned un = ((i + 2 + rotS) & 7) * 2 + (i >= 22);
          const GAS char* sp_ = (const GAS char*)(un & 8 ? Cim : Cre) + co + (un & 7) * 128;
          W.ge(vfp + ((i - 10) >> 1) * 16, 1);
          TRX(2, i);
          WAIT_VM(27);
          B_ITEM(sp_, db, i);
          db = db == (RB2 - 1) * 8192 ? 0 : db + 8192;
        }
      } else {
        const unsigned tag = lds_ld1(L_TAG);
        const unsigned l2 = lane & 3;
        const unsigned fo = WS_FLAG + m * 8192 + (lane & 16 ? 2048 + c16 * 128 : lane & 8 ? 6144 + (l2 * 4 + cp) * 128 : lane & 4 ? 4096 + (l2 * 4 + 3) * 128 : (kap * 4 + l2) * 128);
        const unsigned full = 0xffff0fffu;
        unsigned seen = 0, done = 0;
#pragma nounroll
        do {
          asm volatile("" ::: "memory");
          const unsigned want = (seen & 15) == 15 ? full & ~seen : 15u & ~seen;
          unsigned v = ~tag;
          if (lane < 32 && ((want >> lane) & 1)) v = __builtin_amdgcn_raw_buffer_load_b32(wsr, fo, 0, 16);
          seen |= (unsigned)__ballot(v == tag);
          const unsigned now = (seen & 15) | (((seen >> 16) == 0xffffu) << 4) | (((seen & 0xf0u) == 0xf0u) << 5) | (((seen & 0xf00u) == 0xf00u) << 6);
          if (now != done) {
            if (lane < 7 && ((now >> lane) & 1)) *(volatile LAS unsigned*)(L + (L_R + lane * 4)) = 1u;
            done = now;
          }
        } while (done != 127u && W.spin());
        if (done != 127u) { if (lane < 7) *(volatile LAS unsigned*)(L + (L_R + lane * 4)) = 1u; }
        else if (bid == 255 && lane == 0) __hip_atomic_store((GAS unsigned*)(ws + WS_CTL), tag, RLX_AGENT);
      }
    } else {
      {
        WAIT_VM(0);
        if (lw == 0) ST(8);
        BAR();
        BAR();
        W.ge(L_DMP + lw * 4, 4);
        Pair P9;
#pragma unroll
        for (int tt = 0; tt < 4; ++tt) {
          const unsigned o = (tt < 3 ? L_DUMP + (lw * 3 + tt) * 2048 : A2OFF + (6 + lw) * 8192) + lane * 16;
          P9.r[tt] = *(const LAS v4f*)(L + o); P9.i[tt] = *(const LAS v4f*)(L + (o + 1024));
        }
        C2 cl, E; ssm_part1(P9, L, 4, lane, cl, E);
        if (lw == 0) { if (lane < 16) *(LAS v2f*)(L + (L_E0 + (64 + lane) * 8)) = (v2f){E.r, E.i}; lds_st(L_EF + 8, 1u); }
        else { W.ge(L_EF + 8, 1); if (m < 15) ssm_publish(L, ws, 4, lane, m, kap * 8 + cp, lds_ld1(L_TAG), E); }
        if (lw == 1) ST(14);
      }
      const unsigned vl = lane * 16;
      unsigned i = nown + par, da = A2OFF + i * 8192;
      const GAS char* abase = ws + WS_ACT + (size_t)(m * 56 + kap * 14) * 8192;
      const GAS char* hbase = ws + WS_H + (size_t)(m * 2 + (kap & 1)) * 16 * 8192;
#pragma unroll 1
      for (; i < 30; i += 2) {
        const unsigned un = ((i + 2 + rotS) & 7) * 2 + (i >= 22);
        unsigned ks = 4 * cp + i; ks = ks >= 14 ? ks - 14 : ks;
        const GAS char* sp_ = i < 14 ? abase + ks * 8192 : hbase + un * 8192;
        const unsigned rw = L_R + (i < 14 ? ks >> 2 : i < 22 ? 4 : 5) * 4;
        W.ge((lane & 4) || i < 8 ? rw : L_FP + ((i - 8) >> 1) * 16 + (lane & 3) * 4, 1);
        TRX(1, i);
        WAIT_VM(18);
        GLDSO(sp_ + vl, L + da, 0, 16); GLDSO(sp_ + vl, L + da, 1024, 16); GLDSO(sp_ + vl, L + da, 2048, 16); GLDSO(sp_ + vl, L + da, 3072, 16);
        GLDSO(sp_ + 4096 + vl, L + da + 4096, 0, 16); GLDSO(sp_ + 4096 + vl, L + da + 4096, 1024, 16); GLDSO(sp_ + 4096 + vl, L + da + 4096, 2048, 16); GLDSO(sp_ + 4096 + vl, L + da + 4096, 3072, 16);
        dma_flag(L_FAB + i * 8, nz);
        da = da >= A2OFF + (RA2 - 2) * 8192 ? da - (RA2 - 2) * 8192 : da + 16384;
      }
    }
    __builtin_amdgcn_endpgm();
  }

  const int th = wave & 1, cg = wave >> 1;
  const int i16 = lane & 15, q = lane >> 4;
  {
    const int tok = m * 128 + (kap * 4 + cp) * 8 + wave;
    const v4u xv = *(const GAS v4u*)((const GAS char*)x + (size_t)tok * 1024 + lane * 16);
    const v4u gv = *(const GAS v4u*)((const GAS char*)gw + lane * 16);
    if (wave >= 6) {
      const int pch = wave == 6 ? kap * 128 + 64 + lane : kap * 128 + cp * 16 + i16;
      const float lr = bf2f(Lre[pch]), li = bf2f(Lim[pch]), ld = bf2f(ldt[pch]);
      const float dt = __bfloat162float(__float2bfloat16(__expf(ld)));
      const float e = __expf(dt * lr);
      const float rev = __builtin_amdgcn_fractf(dt * li * 0.15915494309189535f);
      const float ar = e * __builtin_amdgcn_cosf(rev), ai = e * __builtin_amdgcn_sinf(rev);
      const float nr = ar - 1.f, den = 2.f * __builtin_amdgcn_rcpf(lr * lr + li * li);
      const C2 l16 = csq(csq(csq(csq((C2){ar, ai})))), l128 = csq(csq(csq(l16)));
      const unsigned la = L_LAM + (wave == 6 ? lane : 64 + i16) * 32;
      *(LAS v4f*)(L + la) = (v4f){ar, ai, (nr * lr + ai * li) * den, (ai * lr - nr * li) * den};
      *(LAS v4f*)(L + (la + 16)) = (v4f){l16.r, l16.i, l128.r, l128.i};
    }
    if (wave == 5) *(LAS unsigned*)(L + L_TAG) = __hip_atomic_load((GAS unsigned*)(ws + WS_CTL), RLX_AGENT) + 1u;
    float s0 = 0.f;
#pragma unroll
    for (int i = 0; i < 4; ++i) { const unsigned a = xv[i], c = gv[i]; s0 = __builtin_amdgcn_fdot2_f32_bf16(__builtin_bit_cast(bf16x2, a), __builtin_bit_cast(bf16x2, c), s0, false); }
    float s1 = s0; swap32(s0, s1); s0 += s1;
    s1 = s0; swap16(s0, s1); s0 += s1;
    s0 += __builtin_bit_cast(float, __builtin_amdgcn_update_dpp(0, __builtin_bit_cast(int, s0), 0x128, 0xf, 0xf, false));
    s0 += __builtin_bit_cast(float, __builtin_amdgcn_update_dpp(0, __builtin_bit_cast(int, s0), 0x124, 0xf, 0xf, false));
    s0 += __builtin_bit_cast(float, __builtin_amdgcn_update_dpp(0, __builtin_bit_cast(int, s0), 0x122, 0xf, 0xf, false));
    s0 += __builtin_bit_cast(float, __builtin_amdgcn_update_dpp(0, __builtin_bit_cast(int, s0), 0x121, 0xf, 0xf, false));
    const float g = __builtin_amdgcn_rcpf(1.f + __expf(-s0));
    if (lane == 0) __hip_atomic_store((GAS unsigned*)(ws + WS_G + tok * 4), __float_as_uint(g), RLX_AGENT);
  }
  if (tid < N_CTL_WORDS) *(LAS unsigned*)(L + (MISC + tid * 4)) = (tid < 2 * nown && !(tid & 1)) ? 1u : 0u;
  const bool swave = mixed && cg >= 2;
  v4f acc[4][4];
  v4f r9 = {}, i9 = {};
#pragma unroll
  for (int a = 0; a < 4; ++a)
#pragma unroll
    for (int b = 0; b < 4; ++b) acc[a][b] = (v4f){};
  {
    const unsigned ro0 = SLOT1 + i16 * 128 + ((q ^ (i16 >> 1)) << 4);
    unsigned so[2] = {ro0 + 49152, (ro0 ^ 64) + 49152};
    bf16x8 xa[4], xb[4], wf[4], sr, si;
    unsigned sb = SLOT1;
    if (!swave) {
      const unsigned qq = i16 >> 2, key = qq | ((q & 1) << 2);
      const unsigned tw = SLOT1 + 16384 + (cg >> 1) * 16384 + (8 * q + qq) * 256 + (lane & 3) * 8;
      unsigned to[4], xo[2][4];
#pragma unroll
      for (int a = 0; a < 4; ++a) {
        to[a] = tw + ((((cg & 1) * 2 + (a >> 1) + (a & 1) * 4) ^ key) << 5);
        xo[0][a] = ro0 + th * 8192 + ((a ^ cg) << 11); xo[1][a] = xo[0][a] ^ 64;
      }
      PIN_ACC();
      WAIT_LGKM_C();
      BAR();
#pragma unroll
      for (int a = 0; a < 4; ++a) { xa[a] = ld_b128(L, xo[0][a]); xo[0][a] += SLOT1; SB(); wf[a] = ld_tr2(L, to[a], 1024); SB(); }
      sr = ld_b128(L, so[0]); si = ld_b128(L, so[0] + 2048); so[0] += SLOT1;
#define F_STEP(XC, XN, KQN, DL) do { \
        _Pragma("unroll") for (int a = 0; a < 4; ++a) { \
          acc[a][0] = MFMA(wf[a], XC[0], acc[a][0]); acc[a][1] = MFMA(wf[a], XC[1], acc[a][1]); \
          SB(); if (KQN || a) { XN[a] = ld_b128(L, xo[KQN][a]); xo[KQN][a] += DL; } SB(); \
          acc[a][2] = MFMA(wf[a], XC[2], acc[a][2]); acc[a][3] = MFMA(wf[a], XC[3], acc[a][3]); \
          SB(); if (!KQN && !a) { WAIT_ALL_C(); BAR(); XN[0] = ld_b128(L, xo[0][0]); xo[0][0] += DL; } \
          wf[a] = ld_tr2(L, to[a] + KQN * 8192, 1024); if (KQN) to[a] += DL; SB(); } \
        r9 = MFMA(XC[0], sr, r9); i9 = MFMA(XC[0], si, i9); \
        SB(); sr = ld_b128(L, so[KQN]); si = ld_b128(L, so[KQN] + 2048); so[KQN] += DL; SB(); } while (0)
#pragma unroll 1
      for (unsigned t = 0; t < 8; ++t) {
        const unsigned dl = sb == 2 * SLOT1 ? 0u - 2 * SLOT1 : SLOT1;
        sb += dl;
        const unsigned dl2 = sb == 2 * SLOT1 ? 0u - 2 * SLOT1 : SLOT1;
        SB();
        F_STEP(xa, xb, 1, dl);
        F_STEP(xb, xa, 0, dl2);
      }
    } else {
      unsigned xo[2] = {ro0 + th * 8192, (ro0 ^ 64) + th * 8192}, wo[2] = {ro0 + 32768 + (cg & 1) * 8192, (ro0 ^ 64) + 32768 + (cg & 1) * 8192};
      bf16x8 x9;
      PIN_ACC();
      WAIT_LGKM_C();
      BAR();
#pragma unroll
      for (int a = 0; a < 4; ++a) { xa[a] = ld_b128(L, xo[0] + a * 2048); SB(); wf[a] = ld_b128(L, wo[0] + a * 2048); SB(); }
      sr = ld_b128(L, so[0]); si = ld_b128(L, so[0] + 2048);
#define S_STEP(XC, XN, KQN) do { \
        _Pragma("unroll") for (int a = 0; a < 4; ++a) { \
          acc[a][0] = MFMA(XC[0], wf[a], acc[a][0]); acc[a][1] = MFMA(XC[1], wf[a], acc[a][1]); \
          SB(); if (KQN || a) XN[a] = ld_b128(L, xo[KQN] + a * 2048); SB(); \
          acc[a][2] = MFMA(XC[2], wf[a], acc[a][2]); acc[a][3] = MFMA(XC[3], wf[a], acc[a][3]); \
          SB(); if (!KQN && !a) { WAIT_ALL_C(); BAR(); XN[0] = ld_b128(L, xo[0]); } \
          wf[a] = ld_b128(L, wo[KQN] + a * 2048); if (a == 1) { x9 = (cg & 1) ? XC[3] : XC[2]; asm volatile("" : "+v"(x9)); } SB(); } \
        r9 = MFMA(x9, sr, r9); i9 = MFMA(x9, si, i9); \
        SB(); sr = ld_b128(L, so[KQN]); si = ld_b128(L, so[KQN] + 2048); SB(); } while (0)
#pragma unroll 1
      for (unsigned t = 0; t < 8; ++t) {
        S_STEP(xa, xb, 1);
        const unsigned dl = sb == 2 * SLOT1 ? 0u - 2 * SLOT1 : SLOT1;
        sb += dl;
        xo[0] += dl; xo[1] += dl; wo[0] += dl; wo[1] += dl; so[0] += dl; so[1] += dl;
        SB();
        S_STEP(xb, xa, 0);
      }
    }
  }
  if (wave == 0) ST(2);
  if (wave == 7) ST(13);
  {
    const unsigned o = (cg < 3 ? L_DUMP + (th * 3 + cg) * 2048 : A2OFF + (6 + th) * 8192) + lane * 16;
    *(LAS v4f*)(L + o) = r9; *(LAS v4f*)(L + (o + 1024)) = i9;
    lds_inc(L_DMP + th * 4);
  }
  GAS char* myflag = ws + WS_FLAG + m * 8192 + (kap * 4 + cp) * 128;
  if (!swave) {
#pragma unroll
    for (int b = 0; b < 4; ++b) {
      float o[8];
#pragma unroll
      for (int e = 0; e < 8; ++e) {
        const float g = acc[(e >> 2) * 2][b][e & 3], u = acc[(e >> 2) * 2 + 1][b][e & 3];
        o[e] = g * __builtin_amdgcn_rcpf(1.f + __builtin_amdgcn_exp2f(g * -1.44269504088896f)) * u;
      }
      const v4u av = {pk_bf16(o[0], o[1]), pk_bf16(o[2], o[3]), pk_bf16(o[4], o[5]), pk_bf16(o[6], o[7])};
      __builtin_amdgcn_raw_buffer_store_b128(av, wsr, WS_ACT + ((m * 56 + kap * 14 + cp * 4 + cg) * 8 + th * 4 + (b ^ cg)) * 1024 + lane * 16, 0, 16);
      *(LAS v4u*)(L + (A2OFF + cg * 8192 + (th * 4 + (b ^ cg)) * 1024 + lane * 16)) = av;
    }
    lds_inc(L_OWN);
  }
  const unsigned tag = lds_ld1(L_TAG);
  if (!swave && cg != 3) { WAIT_VM(0); arrive(myflag, 0, 2 * nown - 1, tag, lane); }
  if (wave == 0) ST(3);

  if (cg < 2) {
    const int wa = wave & 1, wb = wave >> 1;
    const unsigned qq = i16 >> 2, g1 = q & 1, hh = q >> 1;
    f32x16 ac[2][2] = {};
    unsigned sa = 0, sbb = 0;
    unsigned vfab = L_FAB + (lane & 3) * 4;
    unsigned fp = L_FP + wave * 4;
    unsigned pv;
#define NEXT_PAIR() const unsigned dA = sa == (RA2 - 2) * 8192 ? 0u - (RA2 - 2) * 8192 : 16384u, dB = sbb == (RB2 - 2) * 8192 ? 0u - (RB2 - 2) * 8192 : 16384u; sa += dA; sbb += dB
#define CHECK_LANDED(SIG) do { if (__any(pv == 0)) { if (SIG) { WAIT_LGKM0(); dma_flag(fp, nz); } W.ge(vfab, 1); } vfab += 16; if (wave == 0) TRX(0, (rfl(vfab) - L_FAB) / 8 - 2); } while (0)
#define PEEK() (pv = *(volatile LAS unsigned*)(L + vfab))
    {
      unsigned aF[2]; aF[0] = A2OFF + ((kap & 1) * 4 + wa * 2 + g1) * 1024 + hh * 256 + i16 * 16; aF[1] = aF[0] ^ 4096;
      unsigned bF[2];
#pragma unroll
      for (int a = 0; a < 2; ++a) bF[a] = (4 * hh + qq) * 256 + (((((a ^ (kap >> 1)) * 4) + wb * 2 + g1) ^ ((qq << 1) | hh)) << 5) + (lane & 3) * 8;
      W.ge(L_OWN, 2 * nown);
      W.ge(vfab, 1); vfab += 16;
      WAIT_LGKM_C();
      bf16x8 af[4], bf[4];
#pragma unroll
      for (int a = 0; a < 4; ++a) { af[a] = ld_b128(L, aF[a & 1] + (a >> 1) * 512); bf[a] = ld_tr2(L, bF[a & 1] + (a >> 1) * 2048, 4096); SB(); }
#define A_STEP(OFF) do { \
        _Pragma("unroll") for (int u = 0; u < 2; ++u) { \
          ac[0][0] = MFMA32(bf[2 * u], af[2 * u], ac[0][0]); \
          ac[1][0] = MFMA32(bf[2 * u], af[2 * u + 1], ac[1][0]); \
          SB(); bf[2 * u] = ld_tr2(L, bF[0] + u * 2048 + OFF, 4096); SB(); \
          ac[0][1] = MFMA32(bf[2 * u + 1], af[2 * u], ac[0][1]); \
          SB(); af[2 * u] = ld_b128(L, aF[0] + u * 512 + OFF); SB(); \
          ac[1][1] = MFMA32(bf[2 * u + 1], af[2 * u + 1], ac[1][1]); \
          SB(); af[2 * u + 1] = ld_b128(L, aF[1] + u * 512 + OFF); bf[2 * u + 1] = ld_tr2(L, bF[1] + u * 2048 + OFF, 4096); SB(); } } while (0)
#pragma unroll 1
      for (unsigned n = 0; n < 7; ++n) {
        PEEK();
        NEXT_PAIR();
        SB();
        A_STEP(8192);
        aF[0] += dA; aF[1] += dA; bF[0] += dB; bF[1] += dB;
        if (n < 6) CHECK_LANDED(true);
        SB();
        A_STEP(0);
        if (n == 6 || !__any(pv == 0)) dma_flag(fp, nz);
        fp += 16;
      }
    }
    if (wave == 0) ST(9);
#pragma unroll
    for (int lq = 1; lq < 4; ++lq)
#pragma unroll
      for (int a = 0; a < 2; ++a) {
        const f32x16 u = ac[lq & 1][lq >> 1];
        __builtin_amdgcn_raw_buffer_store_b128((v4u){pk_bf16(u[8 * a], u[8 * a + 1]), pk_bf16(u[8 * a + 2], u[8 * a + 3]), pk_bf16(u[8 * a + 4], u[8 * a + 5]), pk_bf16(u[8 * a + 6], u[8 * a + 7])}, wsr,
            WS_PART + ((((m * 4 + cp) * 4 + (kap ^ lq)) * 3 + (lq - 1)) * 8 + wave * 2 + a) * 1024 + lane * 16, 0, 16);
      }
    const int tok = m * 128 + (kap & 1) * 64 + 16 * (i16 >> 2) + (i16 & 3) + 8 * wa + 4 * g1;
    const int d0 = cp * 128 + (kap >> 1) * 64 + wb * 32 + 4 * hh;
    f32x16 as = {};
    unsigned aS = A2OFF + sa + (wa * 2 + g1) * 2048 + (((qq + 4 * hh) ^ (g1 * 4)) * 32) + (lane & 3) * 8;
    unsigned bS[4];
#pragma unroll
    for (int u = 0; u < 4; ++u) bS[u] = sbb + (wb * 32 + (lane & 31)) * 128 + (((2 * u + hh) ^ ((lane >> 1) & 7)) << 4);
    v4u pt[3][2]; v2u xsk[4], dsk[4]; float gt;
    bf16x8 hf[4], cf[4];
#define B_STEP(OFF, FETCH) do { \
      _Pragma("unroll") for (int u = 0; u < 4; ++u) { \
        as = MFMA32(cf[u], hf[u], as); \
        SB(); if (FETCH) { hf[u] = ld_tr2(L, aS + u * 512 + OFF, 256); cf[u] = ld_b128(L, bS[u] + OFF); } SB(); } } while (0)
#define B_PAIR(SIG, LAST) do { \
      if (!(LAST)) PEEK(); \
      NEXT_PAIR(); \
      SB(); \
      B_STEP(8192, true); \
      aS += dA; bS[0] += dB; bS[1] += dB; bS[2] += dB; bS[3] += dB; \
      if (!(LAST)) CHECK_LANDED(SIG); \
      SB(); \
      B_STEP(0, !(LAST)); \
      if (SIG) { if (!__any(pv == 0)) dma_flag(fp, nz); fp += 16; } } while (0)
    PEEK();
    const bool early = __any(pv == 0);
    if (early) { WAIT_VM(0); arrive(myflag, 3, 3u, tag, lane); }
    CHECK_LANDED(false);
#pragma unroll
    for (int u = 0; u < 4; ++u) { hf[u] = ld_tr2(L, aS + u * 512, 256); cf[u] = ld_b128(L, bS[u]); SB(); }
#pragma unroll 1
    for (unsigned n = 0; n < 6; ++n) {
      B_PAIR(true, false);
      if (n == 1 && !early) { WAIT_VM(2); arrive(myflag, 3, 3u, tag, lane); }
    }
    W.ge(L_R + 24, 1);
#pragma unroll
    for (int s = 0; s < 3; ++s)
#pragma unroll
      for (int a = 0; a < 2; ++a) pt[s][a] = __builtin_amdgcn_raw_buffer_load_b128(wsr, WS_PART + ((((m * 4 + cp) * 4 + kap) * 3 + s) * 8 + wave * 2 + a) * 1024 + lane * 16, 0, 16);
#pragma unroll
    for (int a = 0; a < 4; ++a) {
      xsk[a] = *(const GAS v2u*)((const GAS char*)x + (size_t)tok * 1024 + (d0 + 8 * a) * 2);
      dsk[a] = *(const GAS v2u*)((const GAS char*)Dsk + (d0 + 8 * a) * 2);
    }
    gt = __uint_as_float(__builtin_amdgcn_raw_buffer_load_b32(wsr, WS_G + tok * 4, 0, 16));
    SB();
    B_PAIR(false, false);
    B_PAIR(false, true);
    const __amdgpu_buffer_rsrc_t outr = __builtin_amdgcn_make_buffer_rsrc((void*)out, 0, 2048 * 512 * 4, 0x00020000);
#pragma unroll
    for (int a = 0; a < 4; ++a) {
      v4f o;
#pragma unroll
      for (int i = 0; i < 4; ++i) {
        const unsigned xw = xsk[a][i >> 1], dw = dsk[a][i >> 1];
        const float xv = __uint_as_float((i & 1) ? (xw & 0xffff0000u) : (xw << 16));
        const float dv = __uint_as_float((i & 1) ? (dw & 0xffff0000u) : (dw << 16));
        float f = ac[0][0][4 * a + i];
#pragma unroll
        for (int s = 0; s < 3; ++s) { const unsigned pw = pt[s][a >> 1][(a & 1) * 2 + (i >> 1)]; f += __uint_as_float((i & 1) ? (pw & 0xffff0000u) : (pw << 16)); }
        o[i] = gt * (as[4 * a + i] + xv * dv) + (1.f - gt) * f;
      }
      __builtin_amdgcn_raw_buffer_store_b128(__builtin_bit_cast(v4u, o), outr, (tok * 512 + d0 + 8 * a) * 4, 0, 16);
    }
    if (wave == 0) ST(7);
    asm volatile("s_endpgm" ::: "memory");
#pragma unroll
    for (int a = 0; a < 4; ++a) asm volatile("" : "=v"(acc[a][0]), "=v"(acc[a][1]), "=v"(acc[a][2]), "=v"(acc[a][3]));
    asm volatile("" : "=v"(r9), "=v"(i9));
  }
  {
    int lane;
    asm volatile("v_mbcnt_lo_u32_b32 %0, -1, 0\n\tv_mbcnt_hi_u32_b32 %0, -1, %0" : "=v"(lane));
    __builtin_amdgcn_s_setprio(2);
    Pair P0, P1; C2 cl0, cl1;
    const int sg0 = kap * 8 + 4 + (cg & 1) * 2, pi0 = (cg & 1) * 2;
    if (swave) {
#pragma unroll
      for (int tt = 0; tt < 4; ++tt) { P0.r[tt] = acc[0][tt]; P0.i[tt] = acc[1][tt]; P1.r[tt] = acc[2][tt]; P1.i[tt] = acc[3][tt]; }
      C2 E0, E1;
      ssm_part1(P0, L, pi0, lane, cl0, E0); ssm_part1(P1, L, pi0 + 1, lane, cl1, E1);
      if (th == 0) {
        if (lane < 16) { *(LAS v2f*)(L + (L_E0 + (pi0 * 16 + lane) * 8)) = (v2f){E0.r, E0.i}; *(LAS v2f*)(L + (L_E0 + (pi0 * 16 + 16 + lane) * 8)) = (v2f){E1.r, E1.i}; }
        lds_st(L_EF + (cg & 1) * 4, 1u);
      } else {
        W.ge(L_EF + (cg & 1) * 4, 1);
        if (m < 15) { ssm_publish(L, ws, pi0, lane, m, sg0, tag, E0); ssm_publish(L, ws, pi0 + 1, lane, m, sg0 + 1, tag, E1); }
      }
      asm volatile("" : "+v"(cl0.r), "+v"(cl0.i), "+v"(cl1.r), "+v"(cl1.i));
      if (wave == 7) TRX(3, 8);
    }
    if (cg == 3) {
      Pair P9; C2 cl9, E;
      W.ge(L_DMP + th * 4, 4);
#pragma unroll
      for (int tt = 0; tt < 3; ++tt) {
        P9.r[tt] = *(const LAS v4f*)(L + (L_DUMP + (th * 3 + tt) * 2048 + lane * 16));
        P9.i[tt] = *(const LAS v4f*)(L + (L_DUMP + (th * 3 + tt) * 2048 + 1024 + lane * 16));
      }
      P9.r[3] = r9; P9.i[3] = i9;
      ssm_part1(P9, L, 4, lane, cl9, E);
      asm volatile("" : "+v"(cl9.r), "+v"(cl9.i));
      if (!swave) { WAIT_VM(0); arrive(myflag, 0, 7u, tag, lane); }
      W.ge(L_EF + 8 + (lane & 1) * 4, 1);
      if (wave == 7) TRX(3, 0);
      ssm_part2(P9, L, wsr, 4, lane, m, th, kap * 8 + cp, ssm_carry(L, 4, lane, th, cl9));
      if (wave == 7) TRX(3, 1);
      WAIT_VM(0); arrive(myflag, 1, 1u, tag, lane);
    }
    if (swave) {
      if (wave == 7) TRX(3, 2);
      W.ge(L_CF + 4, 1);
      if (wave == 7) TRX(3, 3);
      ssm_part2(P0, L, wsr, pi0, lane, m, th, sg0, ssm_carry(L, pi0, lane, th, cl0));
      if (wave == 7) TRX(3, 4);
      ssm_part2(P1, L, wsr, pi0 + 1, lane, m, th, sg0 + 1, ssm_carry(L, pi0 + 1, lane, th, cl1));
      if (wave == 7) TRX(3, 5);
      WAIT_VM(0); arrive(myflag, 2, 3u, tag, lane);
      if (wave == 7) TRX(3, 6);
    }
    if (wave == 7) ST(4);
  }
}

extern "C" void kernel_launch(void* const* d_in, const int* in_sizes, int n_in, void* d_out, int out_size, void* d_ws, size_t ws_size, hipStream_t stream) {
  (void)in_sizes; (void)n_in; (void)out_size; (void)ws_size;
  (void)hipFuncSetAttribute((const void*)fused_kernel, hipFuncAttributeMaxDynamicSharedMemorySize, LDS_BYTES);
  fused_kernel<<<256, 768, LDS_BYTES, stream>>>((const u16*)d_in[0], (const u16*)d_in[1], (const u16*)d_in[2], (const u16*)d_in[3], (const u16*)d_in[4],
                                                (const u16*)d_in[5], (const u16*)d_in[6], (const u16*)d_in[7], (const u16*)d_in[8], (const u16*)d_in[10],
                                                (const u16*)d_in[11], (const u16*)d_in[12], (const u16*)d_in[13], (float*)d_out, (char*)d_ws);
}
```

```cpp
#include <hip/hip_runtime.h>

#define GAS __attribute__((address_space(1)))
#define LAS __attribute__((address_space(3)))
#define DI __device__ __forceinline__
typedef unsigned short u16;
typedef unsigned long long u64;
typedef unsigned v4u __attribute__((ext_vector_type(4)));
typedef unsigned v16u __attribute__((ext_vector_type(16)));
typedef unsigned v2u __attribute__((ext_vector_type(2)));
typedef float v4f __attribute__((ext_vector_type(4)));
typedef float v2f __attribute__((ext_vector_type(2)));
typedef short s16x4 __attribute__((ext_vector_type(4)));
typedef __bf16 bf16x8 __attribute__((ext_vector_type(8)));
typedef float f32x16 __attribute__((ext_vector_type(16)));
typedef __bf16 bf16x2 __attribute__((ext_vector_type(2)));

constexpr unsigned WS_CTL  = 0;
constexpr unsigned WS_FLAG = 4096;
constexpr unsigned WS_GRAN = WS_FLAG + 524288;
constexpr unsigned WS_G    = WS_GRAN + 262144;
constexpr unsigned WS_ACT  = 2u << 20;
constexpr unsigned WS_H    = WS_ACT + 16u * 56u * 8192u;
constexpr unsigned WS_PART = WS_H + 16u * 2u * 16u * 8192u;
constexpr unsigned WS_END  = WS_PART + 256u * 3u * 8192u;

constexpr unsigned SLOT1 = 53248;
constexpr unsigned RB2 = 10, RA2 = 8;
constexpr unsigned A2OFF = RB2 * 8192;
constexpr unsigned L_DUMP = A2OFF + RA2 * 8192;
constexpr unsigned MISC  = 3 * SLOT1;
constexpr unsigned L_FAB  = MISC + 0;
constexpr unsigned L_FP   = MISC + 256;
constexpr unsigned L_R    = MISC + 512;
constexpr unsigned L_OWN  = MISC + 524;
constexpr unsigned L_DMP  = MISC + 532;
constexpr unsigned L_CNT  = MISC + 544;
constexpr unsigned N_CTL_WORDS = 160;
constexpr unsigned L_TAG  = MISC + 640;
constexpr unsigned L_SINK = MISC + 704;
constexpr unsigned L_LAM  = MISC + 1024;
constexpr unsigned LDS_BYTES = 163840;

constexpr u64 korder(int cp) {
  u64 r = 0;
  int n = 0;
  for (int i = 0; i < (cp == 3 ? 2 : 4); ++i) r |= (u64)(4 * cp + i) << (4 * n++);
  for (int d = 1; d < 4; ++d) { const int c = d - 1 + (d - 1 >= cp ? 1 : 0); for (int i = 0; i < (c == 3 ? 2 : 4); ++i) r |= (u64)(4 * c + i) << (4 * n++); }
  return r;
}
constexpr u64 KORD0 = korder(0), KORD1 = korder(1), KORD2 = korder(2), KORD3 = korder(3);
constexpr unsigned CODE_BYTES = 24832;
constexpr unsigned SPIN_CAP = 1u << 20;

DI float u2f(unsigned v) { return __builtin_bit_cast(float, v); }
DI unsigned f2u(float v) { return __builtin_bit_cast(unsigned, v); }
DI float fexp(float v) { return __builtin_amdgcn_exp2f(v * 1.44269504088896f); }
DI u64 ballot(bool p) { return __builtin_amdgcn_ballot_w64(p); }
DI float bf2f(unsigned v) { return u2f(v << 16); }
DI unsigned pk_bf16(float a, float b) { v2f p = {a, b}; return __builtin_bit_cast(unsigned, __builtin_convertvector(p, bf16x2)); }
DI unsigned rfl(unsigned v) { return __builtin_amdgcn_readfirstlane(v); }

DI void lds_st(unsigned addr, unsigned v) {
  asm volatile("s_mov_b64 exec, 1\n\tds_write_b32 %0, %1\n\ts_mov_b64 exec, -1" ::"v"(addr), "v"(v) : "memory");
}
DI unsigned lds_ld1(unsigned addr) { unsigned r; asm volatile("ds_read_b32 %0, %1\n\ts_waitcnt lgkmcnt(0)" : "=&v"(r) : "v"(addr) : "memory"); return rfl(r); }
DI void dma_flag(unsigned lds_addr, u64 src) {
  unsigned keep;
  asm volatile("s_mov_b32 %0, m0\n\ts_mov_b32 m0, %1\n\ts_mov_b64 exec, 1\n\tglobal_load_lds_dword %2, %3\n\ts_mov_b64 exec, -1\n\ts_mov_b32 m0, %0"
               : "=&s"(keep) : "s"(lds_addr), "v"(0u), "s"(src) : "memory");
}
DI void lds_inc(unsigned addr) {
  u64 ex;
  asm volatile("s_mov_b64 %0, exec\n\ts_mov_b64 exec, 1\n\tds_add_u32 %1, %2\n\ts_mov_b64 exec, %0" : "=&s"(ex) : "v"(addr), "v"(1u) : "memory");
}
#define WAIT_VM(n) asm volatile("s_waitcnt vmcnt(%0)" ::"i"(n) : "memory")
#define WAIT_LGKM0() asm volatile("s_waitcnt lgkmcnt(0)" ::: "memory")

struct Wait {
  unsigned budget;
  DI void ge(unsigned vaddr, unsigned need) {
    unsigned t; budget = rfl(budget);
    asm volatile("1:\n\tds_read_b32 %0, %2\n\ts_waitcnt lgkmcnt(0)\n\tv_cmp_gt_u32 vcc, %3, %0\n\ts_cbranch_vccz 2f\n\ts_sleep 1\n\t"
                 "s_sub_u32 %1, %1, 1\n\ts_cbranch_scc0 1b\n\ts_mov_b32 %1, 0\n2:"
                 : "=&v"(t), "+s"(budget) : "v"(vaddr), "s"(need) : "memory", "vcc", "scc");
  }
  template <int N> DI void flags(const GAS char* p, unsigned tag) {
#pragma nounroll
    for (;;) {
      unsigned bad;
      if constexpr (N == 4) { v4u v; asm volatile("s_load_dwordx4 %0, %1, 0x0 glc\n\ts_waitcnt lgkmcnt(0)" : "=&s"(v) : "s"(p) : "memory"); bad = (v.x ^ tag) | (v.y ^ tag) | (v.z ^ tag) | (v.w ^ tag); }
      else { v16u v; asm volatile("s_load_dwordx16 %0, %1, 0x0 glc\n\ts_waitcnt lgkmcnt(0)" : "=&s"(v) : "s"(p) : "memory"); bad = 0;
#pragma unroll
        for (int i = 0; i < 16; ++i) bad |= v[i] ^ tag; }
      if (bad == 0 || !spin()) break;
    }
  }
  DI bool spin() { __builtin_amdgcn_s_sleep(1); if (budget == 0) return false; --budget; return true; }
};

DI float op(float v) { asm("" : "+v"(v)); return v; }
DI v2f pmul(v2f a, v2f b) { return a * b; }
DI v2f padd(v2f a, v2f b) { return a + b; }
DI v2f pfma(v2f a, v2f b, v2f c) { return __builtin_elementwise_fma(a, b, c); }
struct C2 { float r, i; };
DI C2 cmul(C2 a, C2 b) { return {op(__builtin_fmaf(a.r, b.r, -op(a.i * b.i))), op(__builtin_fmaf(a.r, b.i, op(a.i * b.r)))}; }
DI C2 cfma(C2 a, C2 b, C2 c) { return {__builtin_fmaf(a.r, b.r, __builtin_fmaf(-a.i, b.i, c.r)), __builtin_fmaf(a.r, b.i, __builtin_fmaf(a.i, b.r, c.i))}; }
DI C2 csq(C2 a) { return {op(__builtin_fmaf(a.r, a.r, -op(a.i * a.i))), op(op(a.r + a.r) * a.i)}; }
DI C2 ocfma(C2 a, C2 b, C2 c) { return {op(__builtin_fmaf(a.r, b.r, op(__builtin_fmaf(-a.i, b.i, c.r)))), op(__builtin_fmaf(a.r, b.i, op(__builtin_fmaf(a.i, b.r, c.i))))}; }
DI C2 csel(bool p, C2 a, C2 b) { return {p ? a.r : b.r, p ? a.i : b.i}; }
DI void swap32(float& x, float& y) { auto r = __builtin_amdgcn_permlane32_swap(f2u(x), f2u(y), false, false); x = u2f(r[0]); y = u2f(r[1]); }
DI void swap16(float& x, float& y) { auto r = __builtin_amdgcn_permlane16_swap(f2u(x), f2u(y), false, false); x = u2f(r[0]); y = u2f(r[1]); }
DI void swap16u(unsigned& x, unsigned& y) { auto r = __builtin_amdgcn_permlane16_swap(x, y, false, false); x = r[0]; y = r[1]; }

#define MFMA(a, b, c) __builtin_amdgcn_mfma_f32_16x16x32_bf16((a), (b), (c), 0, 0, 0)
#define MFMA32(a, b, c) __builtin_amdgcn_mfma_f32_32x32x16_bf16((a), (b), (c), 0, 0, 0)
#define GLDS(g, l, aux) __builtin_amdgcn_global_load_lds((const GAS void*)(g), (LAS void*)(l), 16, 0, (aux))
#define GLDSO(g, l, off, aux) __builtin_amdgcn_global_load_lds((const GAS void*)(g), (LAS void*)(l), 16, (off), (aux))
#define SB() __builtin_amdgcn_sched_barrier(0)
#define RLX_AGENT __ATOMIC_RELAXED, __HIP_MEMORY_SCOPE_AGENT

struct LdsBase { DI LAS char* operator+(unsigned off) const { return (LAS char*)(size_t)off; } };
DI bf16x8 ld_b128(LdsBase L, unsigned off) { return *(const LAS bf16x8*)(L + off); }
DI bf16x8 ld_tr2(LdsBase L, unsigned off, unsigned d) {
  s16x4 a = __builtin_amdgcn_ds_read_tr16_b64_v4i16((LAS s16x4*)(L + off));
  s16x4 b = __builtin_amdgcn_ds_read_tr16_b64_v4i16((LAS s16x4*)(L + off + d));
  return __builtin_bit_cast(bf16x8, __builtin_shufflevector(a, b, 0, 1, 2, 3, 4, 5, 6, 7));
}

DI s16x4 ld_tr(LdsBase L, unsigned off) { return __builtin_amdgcn_ds_read_tr16_b64_v4i16((LAS s16x4*)(L + off)); }
DI bf16x8 cat(s16x4 a, s16x4 b) { return __builtin_bit_cast(bf16x8, __builtin_shufflevector(a, b, 0, 1, 2, 3, 4, 5, 6, 7)); }


DI void arrive(GAS char* flagm, int grp, int line, int word, unsigned last, unsigned tag, int lane) {
  unsigned r;
  asm volatile("s_mov_b64 exec, 1\n\tds_add_rtn_u32 %0, %1, %2\n\ts_waitcnt lgkmcnt(0)\n\ts_mov_b64 exec, -1" : "=&v"(r) : "v"(L_CNT + grp * 4), "v"(1u) : "memory");
  if (rfl(r) == last && lane == 0) __hip_atomic_store((GAS unsigned*)(flagm + line * 128 + word * 4), tag, RLX_AGENT);
}
#define BAR() do { asm volatile("" ::: "memory"); __builtin_amdgcn_s_barrier(); asm volatile("" ::: "memory"); } while (0)
#define WAIT_LGKM_C() __builtin_amdgcn_s_waitcnt(0xC07F)

#define PIN_ACC() do { _Pragma("unroll") for (int a_ = 0; a_ < 4; ++a_) asm volatile("" : "+v"(acc[a_][0]), "+v"(acc[a_][1]), "+v"(acc[a_][2]), "+v"(acc[a_][3])); asm volatile("" : "+v"(r9), "+v"(i9)); } while (0)
DI v4f chan_const(unsigned lrb, unsigned lib, unsigned ldb) {
  const float lr = bf2f(lrb), li = bf2f(lib);
  const float dt = bf2f(pk_bf16(fexp(bf2f(ldb)), 0.f) & 0xffffu);
  const float e = fexp(dt * lr);
  const float turns = dt * li * 0.15915494309189535f, rev = turns - __builtin_rintf(turns);
  const float ar = op(e * __builtin_amdgcn_cosf(rev)), ai = op(e * __builtin_amdgcn_sinf(rev));
  const float nr = ar - 1.f, den = 2.f * __builtin_amdgcn_rcpf(lr * lr + li * li);
  return (v4f){ar, ai, op(op(nr * lr + op(ai * li)) * den), op(op(ai * lr - op(nr * li)) * den)};
}
DI v4f chan_pow(v4f lc) {
  const C2 l16 = csq(csq(csq(csq((C2){lc.x, lc.y})))), l64 = csq(csq(l16));
  return (v4f){l16.r, l16.i, l64.r, l64.i};
}
struct Pair { v4f r[4], i[4]; };

template <int NP>
DI void ssm_flow(Pair (&P)[NP], const v4f (&lc)[NP], const v4f (&lp)[NP], GAS char* ws, __amdgpu_buffer_rsrc_t wsr, Wait& W, int lane, int ch, int sig, unsigned tag) {
  const int c = lane & 15, q = lane >> 4;
  C2 cl[NP];
#pragma unroll
  for (int p = 0; p < NP; ++p) {
    const C2 l1 = {lc[p].x, lc[p].y}, l16 = {lp[p].x, lp[p].y}, l32 = csq(l16);
    C2 e = {P[p].r[0][0], P[p].i[0][0]};
#pragma unroll
    for (int n = 1; n < 16; ++n) e = cfma(l1, e, (C2){P[p].r[n >> 2][n & 3], P[p].i[n >> 2][n & 3]});
    C2 eE = e, eO = e;
    swap16(eE.r, eO.r); swap16(eE.i, eO.i);
    C2 Ulo = cfma(l16, eE, eO), Uhi = Ulo;
    swap32(Ulo.r, Uhi.r); swap32(Ulo.i, Uhi.i);
    const C2 E = cfma(l32, Ulo, Uhi);
    if (lane < 32) __hip_atomic_store((GAS u64*)(ws + WS_GRAN + ((ch * 32 + sig + p) * 16 + c) * 16 + q * 8), ((u64)tag << 32) | f2u(lane & 16 ? E.i : E.r), RLX_AGENT);
    const C2 c0 = csel(lane & 32, Ulo, (C2){0.f, 0.f});
    cl[p] = csel(lane & 16, ocfma(l16, c0, eE), c0);
  }
  SB();
  __builtin_amdgcn_s_setprio(0);
#pragma unroll
  for (int p = 0; p < NP; ++p) {
    v2f cr = {lc[p].z, lc[p].z}, nci = {-lc[p].w, -lc[p].w}, ncr = {-lc[p].z, -lc[p].z};
    asm volatile("" : "+v"(cr), "+v"(nci), "+v"(ncr));
#pragma unroll
    for (int tt = 0; tt < 4; ++tt) {
      const v4f r4 = P[p].r[tt], i4 = P[p].i[tt];
      const v2f r0 = __builtin_shufflevector(r4, r4, 0, 1), r1 = __builtin_shufflevector(r4, r4, 2, 3), i0 = __builtin_shufflevector(i4, i4, 0, 1), i1 = __builtin_shufflevector(i4, i4, 2, 3);
      const v2f a0 = pfma(nci, i0, pmul(cr, r0)), a1 = pfma(nci, i1, pmul(cr, r1)), b0 = pfma(ncr, i0, pmul(nci, r0)), b1 = pfma(ncr, i1, pmul(nci, r1));
      P[p].r[tt] = __builtin_shufflevector(a0, a1, 0, 1, 2, 3); P[p].i[tt] = __builtin_shufflevector(b0, b1, 0, 1, 2, 3);
    }
  }
#pragma unroll
  for (int p = 0; p < NP; ++p)
#pragma unroll
    for (int tt = 0; tt < 4; ++tt) asm volatile("" : "+v"(P[p].r[tt]), "+v"(P[p].i[tt]));
  v4u gv[NP][8] = {};
  if (ch > 0) {
    const int base = WS_GRAN + (sig * 16 + c) * 16 + (ch - 29 - q) * 8192;
    unsigned todo = (1u << ((ch + 3) >> 2)) - 1u;
#pragma nounroll
    for (;;) {
      asm volatile("" ::: "memory");
#pragma unroll
      for (int k = 0; k < 8; ++k)
        if (todo & (1u << k)) {
#pragma unroll
          for (int p = 0; p < NP; ++p) gv[p][k] = __builtin_amdgcn_raw_buffer_load_b128(wsr, base + p * 256, (7 - k) * 32768, 16);
        }
      unsigned left = 0;
#pragma unroll
      for (int k = 0; k < 8; ++k) {
        unsigned bad = 0;
#pragma unroll
        for (int p = 0; p < NP; ++p) bad |= (gv[p][k].y ^ tag) | (gv[p][k].w ^ tag);
        const u64 vm = 4 * k + 3 < ch ? ~0ull : 4 * k >= ch ? 0ull : (1ull << (16 * (ch - 4 * k))) - 1ull;
        left |= ((ballot(bad != 0) & vm) != 0) << k;
      }
      todo = left;
      if (todo == 0 || !W.spin()) break;
      __builtin_amdgcn_s_sleep(3);
    }
  }
  __builtin_amdgcn_s_setprio(3);
  const int pc = (c & 3) | ((c >> 1) & 4) | ((c << 1) & 8);
#pragma unroll
  for (int p = 0; p < NP; ++p) {
    const C2 l16 = {lp[p].x, lp[p].y}, l32 = csq(l16), l64 = {lp[p].z, lp[p].w}, l128 = csq(l64), l256 = csq(l128);
    C2 acc = {0.f, 0.f};
#pragma unroll
    for (int k = 7; k >= 0; --k) {
      const bool valid = (ch - 1 - (4 * k + q)) >= 0;
      acc = cfma(l256, acc, (C2){valid ? u2f(gv[p][k].x) : 0.f, valid ? u2f(gv[p][k].z) : 0.f});
    }
    acc = csel(lane & 16, cmul(acc, l64), acc);
    acc = csel(lane & 32, cmul(acc, l128), acc);
    C2 o = acc; swap16(acc.r, o.r); swap16(acc.i, o.i); acc.r += o.r; acc.i += o.i;
    o = acc; swap32(acc.r, o.r); swap32(acc.i, o.i); acc.r += o.r; acc.i += o.i;
    acc = csel(lane & 16, cmul(acc, l16), acc);
    acc = csel(lane & 32, cmul(acc, l32), acc);
    acc.r += cl[p].r; acc.i += cl[p].i;
    C2 s = cmul((C2){lc[p].z, lc[p].w}, acc); s.i = -s.i;
    const C2 lb = {lc[p].x, -lc[p].y};
    const int sg = sig + p;
    const unsigned off = WS_H + (ch * 16 + (q & 1) * 8 + (sg >> 2)) * 8192 + ((sg >> 1) & 1) * 1024 + (sg & 1) * 512 + pc * 32 + (q >> 1) * 16;
#pragma unroll
    for (int tt = 0; tt < 4; ++tt) {
      C2 h[4];
#pragma unroll
      for (int k = 0; k < 4; ++k) { s = cfma(lb, s, (C2){P[p].r[tt][k], P[p].i[tt][k]}); h[k] = s; }
      unsigned a0 = pk_bf16(h[0].r, h[1].r), a1 = pk_bf16(h[2].r, h[3].r), b0 = pk_bf16(h[0].i, h[1].i), b1 = pk_bf16(h[2].i, h[3].i);
      swap16u(a0, b0); swap16u(a1, b1);
      __builtin_amdgcn_raw_buffer_store_b128((v4u){a0, a1, b0, b1}, wsr, (off ^ ((tt & 1) * 128)) + tt * 2048, 0, 16);
    }
  }
}

extern "C" __global__ void __launch_bounds__(768, 3)
fused_kernel(const u16* __restrict__ x, const u16* __restrict__ Lre, const u16* __restrict__ Lim, const u16* __restrict__ Bre,
             const u16* __restrict__ Bim, const u16* __restrict__ Cre, const u16* __restrict__ Cim, const u16* __restrict__ ldt,
             const u16* __restrict__ Dsk, const u16* __restrict__ wg, const u16* __restrict__ wu, const u16* __restrict__ wd,
             const u16* __restrict__ gw, float* __restrict__ out, char* wsp) {
  const LdsBase L;
  asm volatile("" :: "s"(x), "s"(Bre), "s"(Bim), "s"(wg), "s"(wu), "s"(gw), "s"(Lre), "s"(Lim), "s"(ldt), "s"(wsp));
  GAS char* ws = (GAS char*)wsp;
  const int tid0 = __builtin_amdgcn_workitem_id_x(), lane = tid0 & 63, hw = rfl(tid0 >> 6), wave = hw - 4, tid = tid0 - 256;
  const int bid = __builtin_amdgcn_workgroup_id_x(), xcd = bid & 7, idx = bid >> 3;
  const int m = (xcd & 1) * 8 + (idx & 7), kap = xcd >> 1, cp = idx >> 3;
  const bool mixed = cp == 3;
  const unsigned nown = mixed ? 2 : 4;
  Wait W; W.budget = SPIN_CAP;
  u64 nz;
  asm volatile("s_getpc_b64 %0\n\ts_nop 0" : "=s"(nz));
#define CODE_TOUCH() do { const unsigned ln_ = idx * 8 + (lane & 7); \
    __builtin_amdgcn_global_load_lds((const GAS void*)((nz & ~255ull) + (ln_ * 128 < CODE_BYTES ? ln_ * 128 : 0)), (LAS void*)(L + L_SINK), 4, 0, 0); } while (0)
  const __amdgpu_buffer_rsrc_t wsr = __builtin_amdgcn_make_buffer_rsrc((void*)wsp, 0, WS_END, 0x00020000);

  if (hw == 4) CODE_TOUCH();
  if (__builtin_expect(hw < 4, 1)) {
    const unsigned lw = hw;
    __builtin_amdgcn_s_setprio(3);
    const unsigned r8 = lane >> 3, c16 = lane & 15, q4 = lane >> 4;
    const unsigned sw0 = ((lane & 7) ^ q4) << 4;
    const unsigned vr0 = r8 * 1024 + sw0, vr1 = vr0 ^ 64;
    const int gtok = m * 128 + (kap * 4 + cp) * 8 + (lw & 1) * 4;
    v4u xv[4], gv; unsigned lrb, lib, ldb, ep;
    if (lw < 2) {
      const int pch = lw == 0 ? kap * 128 + 64 + lane : kap * 128 + cp * 16 + c16;
      lrb = Lre[pch]; lib = Lim[pch]; ldb = ldt[pch];
      ep = __builtin_amdgcn_raw_buffer_load_b32(wsr, WS_CTL, 0, 16);
    } else {
#pragma unroll
      for (int k = 0; k < 4; ++k) xv[k] = *(const GAS v4u*)((const GAS char*)x + (size_t)(gtok + k) * 1024 + lane * 16);
      gv = *(const GAS v4u*)((const GAS char*)gw + lane * 16);
    }
    asm volatile("" ::: "memory");
    {
      const unsigned vx0 = (16 * (r8 >> 2) + (r8 & 3)) * 1024 + sw0;
      const unsigned vxo[4] = {vx0, (vx0 ^ 64) + 31 * 1024, vx0 + 2 * 1024, (vx0 ^ 64) + 33 * 1024};
      const GAS char* xp = (const GAS char*)x + (size_t)(m * 128 + (lw >> 1) * 64 + (lw & 1) * 8) * 1024;
      const bool sl = mixed && lw >= 2;
      const GAS char* wp = sl ? (const GAS char*)(lw & 1 ? Bim : Bre) + (kap * 128 + 64) * 1024 : (const GAS char*)(lw & 1 ? wu : wg) + kap * 896 + cp * 256 + (lw >> 1) * 128;
      const GAS char* bp = (const GAS char*)(lw < 2 ? Bre : Bim) + (size_t)(kap * 128 + cp * 16 + (lw & 1) * 8) * 1024;
      const unsigned wst = sl ? 128 : 64 * 3584, rs = sl ? 1024 : 3584;
      const unsigned vb = (lw & 1) ? vr1 : vr0;
      unsigned vw[8];
      unsigned sbase = SLOT1, tb = 0;
#pragma unroll 1
      for (unsigned t = 0; t < 8; ++t) {
        LAS char* lp = L + (sbase + lw * 4096);
        const GAS char* xk = xp + t * 128;
        GLDSO(xk + vxo[0], lp, 0, 0); GLDSO(xk + vxo[1], lp, 1024, 0); GLDSO(xk + vxo[2], lp, 2048, 0); GLDSO(xk + vxo[3], lp, 3072, 0);
        if (t == 0) {
          const unsigned w0 = r8 * rs + (sl ? sw0 : ((((lane & 7) >> 1) ^ ((r8 >> 1) & 1)) << 5) + (lane & 1) * 16);
#pragma unroll
          for (int p = 0; p < 8; ++p) vw[p] = ((p & 1) ? (w0 ^ 64) : w0) + p * 8 * rs - (p & 3) * 1024;
        }
        LAS char* lq = L + (sbase + 16384 + lw * 8192);
        const GAS char* wk = wp + t * wst;
        GLDSO(wk + vw[0], lq, 0, 0); GLDSO(wk + vw[1], lq, 1024, 0); GLDSO(wk + vw[2], lq, 2048, 0); GLDSO(wk + vw[3], lq, 3072, 0);
        GLDSO(wk + vw[4], lq + 4096, 0, 0); GLDSO(wk + vw[5], lq + 4096, 1024, 0); GLDSO(wk + vw[6], lq + 4096, 2048, 0); GLDSO(wk + vw[7], lq + 4096, 3072, 0);
        GLDS(bp + t * 128 + vb, L + (sbase + 49152 + lw * 1024), 0);
        if (t == 4 && lw == 3) CODE_TOUCH();
        sbase += SLOT1; if (sbase == 3 * SLOT1) sbase = 0;
        if (t > 0) {
          WAIT_VM(13);
          if (t == 2 && lw == 3) while ((unsigned)__builtin_amdgcn_s_memrealtime() - tb < 95u) __builtin_amdgcn_s_sleep(1);
          BAR();
          if (t == 1) tb = (unsigned)__builtin_amdgcn_s_memrealtime();
        }
      }
    }
    const unsigned par = lw & 1;
    const unsigned rotS = m & 7;
    const u64 kord = cp == 0 ? KORD0 : cp == 1 ? KORD1 : cp == 2 ? KORD2 : KORD3;
    GAS char* flagm = ws + WS_FLAG + m * 2048;
    if (lw >= 2) {
      unsigned vo[8];
      {
        const unsigned v0 = q4 * 1024 + (((c16 >> 1) ^ (q4 << 1)) << 5) + (c16 & 1) * 16;
#pragma unroll
        for (int p = 0; p < 8; ++p) vo[p] = ((p & 1) ? (v0 ^ 32) : v0) + p * 4096 - (p & 3) * 1024;
      }
      const GAS char* bbase = (const GAS char*)wd + (size_t)kap * 448 * 1024 + cp * 256;
#define B_ITEM(sp_, db, i) do { \
        GLDSO(sp_ + vo[0], L + db, 0, 0); GLDSO(sp_ + vo[1], L + db, 1024, 0); GLDSO(sp_ + vo[2], L + db, 2048, 0); GLDSO(sp_ + vo[3], L + db, 3072, 0); \
        GLDSO(sp_ + vo[4], L + db + 4096, 0, 0); GLDSO(sp_ + vo[5], L + db + 4096, 1024, 0); GLDSO(sp_ + vo[6], L + db + 4096, 2048, 0); GLDSO(sp_ + vo[7], L + db + 4096, 3072, 0); \
        dma_flag(L_FAB + (i) * 8 + 4, nz); } while (0)
      unsigned i = par;
#pragma unroll 1
      for (; i < 10; i += 2) {
        const GAS char* sp_ = bbase + (unsigned)((kord >> (4 * i)) & 15) * 32768;
        B_ITEM(sp_, i * 8192, i);
        if ((i >> 1) == 1) { WAIT_VM(18); BAR(); }
        if ((i >> 1) == 3) BAR();
      }
      const unsigned tag = lds_ld1(L_TAG);
      asm volatile("" : "+v"(gv));
      {
        float sg[4];
#pragma unroll
        for (int k = 0; k < 4; ++k) {
          sg[k] = 0.f;
#pragma unroll
          for (int i = 0; i < 4; ++i) { const unsigned a = xv[k][i], c = gv[i]; sg[k] = __builtin_amdgcn_fdot2_f32_bf16(__builtin_bit_cast(bf16x2, a), __builtin_bit_cast(bf16x2, c), sg[k], false); }
        }
        swap32(sg[0], sg[2]); swap32(sg[1], sg[3]); sg[0] += sg[2]; sg[1] += sg[3];
        swap16(sg[0], sg[1]); float s0 = sg[0] + sg[1];
        s0 += __builtin_bit_cast(float, __builtin_amdgcn_update_dpp(0, __builtin_bit_cast(int, s0), 0x128, 0xf, 0xf, false));
        s0 += __builtin_bit_cast(float, __builtin_amdgcn_update_dpp(0, __builtin_bit_cast(int, s0), 0x124, 0xf, 0xf, false));
        s0 += __builtin_bit_cast(float, __builtin_amdgcn_update_dpp(0, __builtin_bit_cast(int, s0), 0x122, 0xf, 0xf, false));
        s0 += __builtin_bit_cast(float, __builtin_amdgcn_update_dpp(0, __builtin_bit_cast(int, s0), 0x121, 0xf, 0xf, false));
        const float g = __builtin_amdgcn_rcpf(1.f + fexp(-s0));
        if (c16 == 0) __hip_atomic_store((GAS unsigned*)(ws + WS_G + (gtok + q4) * 4), f2u(g), RLX_AGENT);
      }
      WAIT_VM(0); arrive(flagm, 1, 4, kap * 4 + cp, 3u, tag, lane);
      const unsigned vfp = L_FP + (lane & 3) * 4;
      if (par == 0) {
        unsigned db = 0;
#pragma unroll 1
        for (i = 10; i < 14; ++i) {
          const GAS char* sp_ = bbase + (unsigned)((kord >> (4 * i)) & 15) * 32768;
          if (!(i & 1)) W.ge(vfp + (i - 10) * 8, 1);
          B_ITEM(sp_, db, i);
          db += 8192;
        }
#pragma unroll
        for (int p = 0; p < 8; ++p) vo[p] = ((p & 1) ? vr1 : vr0) + p * 8192 - (p & 3) * 1024;
        const unsigned co = (cp * 128 + (kap >> 1) * 64) * 1024;
#pragma unroll 1
        for (; i < 30; ++i) {
          const unsigned un = ((i + 2 + rotS) & 7) * 2 + (i >= 22);
          const GAS char* sp_ = (const GAS char*)(un & 8 ? Cim : Cre) + co + (un & 7) * 128;
          if (!(i & 1)) { W.ge(vfp + (i - 10) * 8, 1); WAIT_VM(18); }
          B_ITEM(sp_, db, i);
          db = db == (RB2 - 1) * 8192 ? 0 : db + 8192;
        }
      } else {
        const unsigned vl = lane * 16;
        const GAS char* abase = ws + WS_ACT + (size_t)(m * 56 + kap * 14) * 8192;
        const GAS char* hbase = ws + WS_H + (size_t)(m * 2 + (kap & 1)) * 16 * 8192;
        unsigned seen = 0, da = A2OFF + nown * 8192;
        W.ge(L_DMP + 8, 2);
#define A_ITEM(sp_, da_, i_) do { \
          GLDSO(sp_ + vl, L + (da_), 0, 16); GLDSO(sp_ + vl, L + (da_), 1024, 16); GLDSO(sp_ + vl, L + (da_), 2048, 16); GLDSO(sp_ + vl, L + (da_), 3072, 16); \
          GLDSO(sp_ + 4096 + vl, L + (da_) + 4096, 0, 16); GLDSO(sp_ + 4096 + vl, L + (da_) + 4096, 1024, 16); GLDSO(sp_ + 4096 + vl, L + (da_) + 4096, 2048, 16); GLDSO(sp_ + 4096 + vl, L + (da_) + 4096, 3072, 16); \
          dma_flag(L_FAB + (i_) * 8, nz); } while (0)
#pragma unroll 1
        for (i = nown; i < 14; i += 2) {
          const unsigned ks = (unsigned)((kord >> (4 * i)) & 15);
#pragma nounroll
          while (!((seen >> (ks >> 2)) & 1)) {
            v4u v; asm volatile("s_load_dwordx4 %0, %1, 0x0 glc\n\ts_waitcnt lgkmcnt(0)" : "=&s"(v) : "s"(flagm + kap * 128) : "memory");
            seen = (v.x == tag) | ((v.y == tag) << 1) | ((v.z == tag) << 2) | ((v.w == tag) << 3);
            if (((seen >> (ks >> 2)) & 1) || !W.spin()) break;
          }
          if (i >= 8) W.ge(vfp + (i - 8) * 8, 1);
          const GAS char* sp_ = abase + ks * 8192;
          WAIT_VM(36);
          A_ITEM(sp_, da, i); A_ITEM(sp_ + 8192, da + 8192, i + 1);
          da = da == A2OFF + (RA2 - 2) * 8192 ? A2OFF : da + 16384;
        }
#pragma unroll 1
        for (; i < 30; i += 2) {
          const unsigned hi = i >= 22;
          W.ge((lane & 4) ? L_R + hi * 4 : vfp + (i - 8) * 8, 1);
          WAIT_VM(36);
          const GAS char* s0_ = hbase + (((i + 2 + rotS) & 7) * 2 + hi) * 8192;
          const GAS char* s1_ = hbase + (((i + 3 + rotS) & 7) * 2 + hi) * 8192;
          A_ITEM(s0_, da, i); A_ITEM(s1_, da + 8192, i + 1);
          da = da == A2OFF + (RA2 - 2) * 8192 ? A2OFF : da + 16384;
        }
      }
    } else {
      WAIT_VM(0);
      asm volatile("" : "+v"(lrb), "+v"(lib), "+v"(ldb), "+v"(ep));
      const unsigned tag = rfl(ep) + 1u;
      {
        v4f c_ = chan_const(lrb, lib, ldb);
        asm volatile("" : "+v"(c_));
        BAR();
        asm volatile("" : "+v"(c_));
        const v4f p_ = chan_pow(c_);
        const unsigned la = L_LAM + (lw == 0 ? lane : 64 + c16) * 32;
        *(LAS v4f*)(L + la) = c_; *(LAS v4f*)(L + (la + 16)) = p_;
        if (lw == 0) lds_st(L_TAG, tag);
        WAIT_LGKM0();
        BAR();
      }
      v4f lc[1], lp[1];
      lc[0] = *(const LAS v4f*)(L + (L_LAM + (64 + c16) * 32)); lp[0] = *(const LAS v4f*)(L + (L_LAM + (64 + c16) * 32 + 16));
      W.ge(L_DMP + lw * 4, 4);
      Pair P9[1];
#pragma unroll
      for (int tt = 0; tt < 4; ++tt) {
        const unsigned o = (tt < 3 ? L_DUMP + (lw * 3 + tt) * 2048 : A2OFF + (6 + lw) * 8192) + lane * 16;
        P9[0].r[tt] = *(const LAS v4f*)(L + o); P9[0].i[tt] = *(const LAS v4f*)(L + (o + 1024));
      }
      lds_inc(L_DMP + 8);
      ssm_flow<1>(P9, lc, lp, ws, wsr, W, lane, m * 2 + lw, kap * 8 + cp, tag);
      WAIT_VM(0); arrive(flagm, 1, 4, kap * 4 + cp, 3u, tag, lane);
      if (lw == 0) {
        W.flags<16>(flagm + 512, tag); lds_st(L_R, 1u);
        if (W.budget != 0 && bid == 255 && lane == 0) __hip_atomic_store((GAS unsigned*)(ws + WS_CTL), tag, RLX_AGENT);
        W.flags<4>(flagm + 1024 + cp * 128, tag); lds_st(L_R + 8, 1u);
      } else { W.flags<4>(flagm + 640, tag); lds_st(L_R + 4, 1u); }
    }
    asm volatile("s_endpgm" ::: "memory");
  }

  const int th = wave & 1, cg = wave >> 1;
  const int i16 = lane & 15, q = lane >> 4;
  if (tid < N_CTL_WORDS) *(LAS unsigned*)(L + (MISC + tid * 4)) = (tid < 2 * nown && !(tid & 1)) ? 1u : 0u;
  const bool swave = mixed && cg >= 2;
  v4f acc[4][4];
  v4f r9 = {}, i9 = {};
#pragma unroll
  for (int a = 0; a < 4; ++a)
#pragma unroll
    for (int b = 0; b < 4; ++b) acc[a][b] = (v4f){};
  {
    const unsigned ro0 = SLOT1 + i16 * 128 + ((q ^ (i16 >> 1)) << 4);
    unsigned so[2] = {ro0 + 49152, (ro0 ^ 64) + 49152};
    bf16x8 xa[4], xb[4], wf[4], sr, si;
    unsigned sb = SLOT1;
    if (!swave) {
      const unsigned qq = i16 >> 2, key = (qq >> 1) | ((q & 1) << 1);
      const unsigned tw = SLOT1 + 16384 + (cg >> 1) * 16384 + (8 * q + qq) * 128 + (lane & 3) * 8;
      unsigned to[2], xo[2][4];
#pragma unroll
      for (int a = 0; a < 2; ++a) to[a] = tw + ((((cg & 1) * 2 + a) ^ key) << 5);
#pragma unroll
      for (int a = 0; a < 4; ++a) { xo[0][a] = ro0 + th * 8192 + ((a ^ cg) << 11); xo[1][a] = xo[0][a] ^ 64; }
      PIN_ACC();
      WAIT_LGKM_C();
      BAR();
#pragma unroll
      for (int a = 0; a < 4; ++a) { xa[a] = ld_b128(L, xo[0][a]); xo[0][a] += SLOT1; SB(); wf[a] = ld_tr2(L, to[a >> 1] + (a & 1) * 8192, 512); SB(); }
      sr = ld_b128(L, so[0]); si = ld_b128(L, so[0] + 2048); so[0] += SLOT1;
#define STAGE_PRIO(KQN, a) do { if (!KQN && a == 2) __builtin_amdgcn_s_setprio(2); if (KQN && a == 0) __builtin_amdgcn_s_setprio(1); if (KQN && a == 2) __builtin_amdgcn_s_setprio(0); } while (0)
#define WA(a, KQN) (to[(a) >> 1] + ((a) & 1) * 8192 + KQN * 4096)
#define F_STEP(XC, XN, KQN, DL) do { s16x4 lo_; \
        _Pragma("unroll") for (int a = 0; a < 4; ++a) { \
          acc[a][0] = MFMA(wf[a], XC[0], acc[a][0]); SB(); \
          if (a > 0) wf[a - 1] = cat(lo_, ld_tr(L, WA(a - 1, KQN) + 512)); SB(); \
          acc[a][1] = MFMA(wf[a], XC[1], acc[a][1]); SB(); \
          if (KQN || a) XN[a] = ld_b128(L, xo[KQN][a]); SB(); \
          acc[a][2] = MFMA(wf[a], XC[2], acc[a][2]); SB(); \
          if (KQN || a) xo[KQN][a] += DL; if (KQN && a == 2) to[0] += DL; if (!KQN && a == 1) XN[0] = ld_b128(L, xo[0][0]); if (!KQN && a == 2) xo[0][0] += DL; SB(); \
          acc[a][3] = MFMA(wf[a], XC[3], acc[a][3]); SB(); \
          if (!KQN && !a) { WAIT_LGKM_C(); BAR(); __builtin_amdgcn_s_setprio(3); } \
          STAGE_PRIO(KQN, a); \
          lo_ = ld_tr(L, WA(a, KQN)); SB(); } \
        r9 = MFMA(XC[0], sr, r9); asm volatile("" : "+v"(r9)); SB(); \
        wf[3] = cat(lo_, ld_tr(L, WA(3, KQN) + 512)); sr = ld_b128(L, so[KQN]); SB(); \
        i9 = MFMA(XC[0], si, i9); asm volatile("" : "+v"(i9)); SB(); \
        si = ld_b128(L, so[KQN] + 2048); so[KQN] += DL; if (KQN) to[1] += DL; SB(); } while (0)
#pragma unroll 1
      for (unsigned t = 0; t < 8; ++t) {
        const unsigned dl = sb == 2 * SLOT1 ? 0u - 2 * SLOT1 : SLOT1;
        sb += dl;
        const unsigned dl2 = sb == 2 * SLOT1 ? 0u - 2 * SLOT1 : SLOT1;
        SB();
        F_STEP(xa, xb, 1, dl);
        F_STEP(xb, xa, 0, dl2);
      }
    } else {
      unsigned xo[2] = {ro0 + th * 8192, (ro0 ^ 64) + th * 8192}, wo[2] = {ro0 + 32768 + (cg & 1) * 4096, (ro0 ^ 64) + 32768 + (cg & 1) * 4096};
      bf16x8 x9;
      PIN_ACC();
      WAIT_LGKM_C();
      BAR();
#pragma unroll
      for (int a = 0; a < 4; ++a) { xa[a] = ld_b128(L, xo[0] + a * 2048); SB(); wf[a] = ld_b128(L, wo[0] + (a >> 1) * 2048 + (a & 1) * 8192); SB(); }
      sr = ld_b128(L, so[0]); si = ld_b128(L, so[0] + 2048);
#define S_STEP(XC, XN, KQN) do { \
        _Pragma("unroll") for (int a = 0; a < 4; ++a) { \
          acc[a][0] = MFMA(XC[0], wf[a], acc[a][0]); SB(); \
          if (!KQN && a == 1) XN[0] = ld_b128(L, xo[0]); SB(); \
          acc[a][1] = MFMA(XC[1], wf[a], acc[a][1]); SB(); \
          if (KQN || a) XN[a] = ld_b128(L, xo[KQN] + a * 2048); SB(); \
          acc[a][2] = MFMA(XC[2], wf[a], acc[a][2]); SB(); \
          if (a == 2) { x9 = (cg & 1) ? XC[3] : XC[2]; asm volatile("" : "+v"(x9)); } SB(); \
          acc[a][3] = MFMA(XC[3], wf[a], acc[a][3]); SB(); \
          if (!KQN && !a) { WAIT_LGKM_C(); BAR(); __builtin_amdgcn_s_setprio(3); } \
          STAGE_PRIO(KQN, a); \
          wf[a] = ld_b128(L, wo[KQN] + (a >> 1) * 2048 + (a & 1) * 8192); SB(); } \
        r9 = MFMA(x9, sr, r9); asm volatile("" : "+v"(r9)); SB(); sr = ld_b128(L, so[KQN]); SB(); \
        i9 = MFMA(x9, si, i9); asm volatile("" : "+v"(i9)); SB(); si = ld_b128(L, so[KQN] + 2048); SB(); } while (0)
#pragma unroll 1
      for (unsigned t = 0; t < 8; ++t) {
        S_STEP(xa, xb, 1);
        const unsigned dl = sb == 2 * SLOT1 ? 0u - 2 * SLOT1 : SLOT1;
        sb += dl;
        xo[0] += dl; xo[1] += dl; wo[0] += dl; wo[1] += dl; so[0] += dl; so[1] += dl;
        SB();
        S_STEP(xb, xa, 0);
      }
    }
  }
  {
    const unsigned o = (cg < 3 ? L_DUMP + (th * 3 + cg) * 2048 : A2OFF + (6 + th) * 8192) + lane * 16;
    *(LAS v4f*)(L + o) = r9; *(LAS v4f*)(L + (o + 1024)) = i9;
    lds_inc(L_DMP + th * 4);
  }
  GAS char* flagm = ws + WS_FLAG + m * 2048;
  if (swave) {
    __builtin_amdgcn_s_setprio(1);
    Pair P[2]; v4f lc[2], lp[2];
#pragma unroll
    for (int p = 0; p < 2; ++p) {
      lc[p] = *(const LAS v4f*)(L + (L_LAM + (((cg & 1) * 2 + p) * 16 + i16) * 32)); lp[p] = *(const LAS v4f*)(L + (L_LAM + (((cg & 1) * 2 + p) * 16 + i16) * 32 + 16));
#pragma unroll
      for (int tt = 0; tt < 4; ++tt) { P[p].r[tt] = acc[2 * p][tt]; P[p].i[tt] = acc[2 * p + 1][tt]; }
    }
    const unsigned tag = rfl(*(const LAS unsigned*)(L + L_TAG));
    ssm_flow<2>(P, lc, lp, ws, wsr, W, lane, m * 2 + th, kap * 8 + 4 + (cg & 1) * 2, tag);
    WAIT_VM(0); arrive(flagm, 2, 5, kap, 3u, tag, lane);
    asm volatile("s_endpgm" ::: "memory");
#pragma unroll
    for (int a = 0; a < 4; ++a) asm volatile("" : "=v"(acc[a][0]), "=v"(acc[a][1]), "=v"(acc[a][2]), "=v"(acc[a][3]));
  }
  {
    v4u av[4];
    v2f kneg = {-1.44269504088896f, -1.44269504088896f}, kone = {1.f, 1.f};
    asm volatile("" : "+v"(kneg), "+v"(kone));
    if (cg >= 2) __builtin_amdgcn_s_setprio(2); else __builtin_amdgcn_s_setprio(1);
    const unsigned go = WS_ACT + ((m * 56 + kap * 14 + cp * 4 + cg) * 8 + th * 4) * 1024 + lane * 16;
#pragma unroll
    for (int b = 0; b < 4; ++b) {
#pragma unroll
      for (int e = 0; e < 4; ++e) {
        const v4f g4 = acc[(e >> 1) * 2][b], u4 = acc[(e >> 1) * 2 + 1][b];
        const v2f g = (e & 1) ? __builtin_shufflevector(g4, g4, 2, 3) : __builtin_shufflevector(g4, g4, 0, 1);
        const v2f u = (e & 1) ? __builtin_shufflevector(u4, u4, 2, 3) : __builtin_shufflevector(u4, u4, 0, 1);
        const v2f t = pmul(g, kneg);
        const v2f d = padd((v2f){__builtin_amdgcn_exp2f(t.x), __builtin_amdgcn_exp2f(t.y)}, kone);
        const v2f o = pmul(pmul(g, u), (v2f){__builtin_amdgcn_rcpf(d.x), __builtin_amdgcn_rcpf(d.y)});
        av[b][e] = pk_bf16(o.x, o.y);
      }
      __builtin_amdgcn_raw_buffer_store_b128(av[b], wsr, go + (b ^ cg) * 1024, 0, 16);
      if (b == 1) __builtin_amdgcn_s_setprio(1);
      *(LAS v4u*)(L + (A2OFF + cg * 8192 + (th * 4 + (b ^ cg)) * 1024 + lane * 16)) = av[b];
    }
    lds_inc(L_OWN + (cg >> 1) * 4);
  }
  const unsigned tag = lds_ld1(L_TAG);
  WAIT_VM(0); arrive(flagm, 0, kap, cp, 2 * nown - 1, tag, lane);

  if (cg < 2) {
    const int wa = wave & 1, wb = wave >> 1;
    const unsigned qq = i16 >> 2, g1 = q & 1, hh = q >> 1;
    f32x16 ac[2][2] = {};
    const int tok = m * 128 + (kap & 1) * 64 + 16 * (i16 >> 2) + (i16 & 3) + 8 * wa + 4 * g1;
    const int d0 = cp * 128 + (kap >> 1) * 64 + wb * 32 + 4 * hh;
    v2u xsk[4], dsk[4];
#pragma unroll
    for (int a = 0; a < 4; ++a) {
      xsk[a] = *(const GAS v2u*)((const GAS char*)x + (size_t)tok * 1024 + (d0 + 8 * a) * 2);
      dsk[a] = *(const GAS v2u*)((const GAS char*)Dsk + (d0 + 8 * a) * 2);
    }
    unsigned sa = 0, sbb = 0;
    unsigned vfab = L_FAB + (lane & 3) * 4;
    unsigned fp = L_FP + wave * 4;
    unsigned pv;
#define NEXT_PAIR() const unsigned dA = sa == (RA2 - 2) * 8192 ? 0u - (RA2 - 2) * 8192 : 16384u, dB = sbb == (RB2 - 2) * 8192 ? 0u - (RB2 - 2) * 8192 : 16384u; sa += dA; sbb += dB
#define SIGNAL() do { lds_st(fp, 1u); fp += 16; } while (0)
#define CHECK_LANDED() do { if (__builtin_expect(ballot(pv == 0) != 0, 0)) W.ge(vfab, 1); vfab += 16; } while (0)
#define PEEK() (pv = *(volatile LAS unsigned*)(L + vfab))
    {
      unsigned aF[2]; aF[0] = A2OFF + ((kap & 1) * 4 + wa * 2 + g1) * 1024 + hh * 256 + i16 * 16; aF[1] = aF[0] ^ 4096;
      unsigned bF[2];
#pragma unroll
      for (int a = 0; a < 2; ++a) bF[a] = (4 * hh + qq) * 256 + (((((a ^ (kap >> 1)) * 4) + wb * 2 + g1) ^ ((qq << 1) | hh)) << 5) + (lane & 3) * 8;
      asm volatile("" : "+v"(ac[0][0]), "+v"(ac[0][1]), "+v"(ac[1][0]), "+v"(ac[1][1]));
      W.ge(L_OWN + (mixed ? 0 : (lane & 1) * 4), 4);
      W.ge(vfab, 1); vfab += 16;
      WAIT_LGKM_C();
      bf16x8 a0[4], b0[4], a1[4], b1[4];
#pragma unroll
      for (int a = 0; a < 4; ++a) { b0[a] = ld_tr2(L, bF[a & 1] + (a >> 1) * 2048, 4096); a0[a] = ld_b128(L, aF[a & 1] + (a >> 1) * 512); SB(); }
#define A_STEP(AC, BC, AN, BN, OFF, C0, C1) do { \
        ac[0][0] = MFMA32(BC[0], AC[0], ac[0][0]); SB(); C0; SB(); \
        ac[1][0] = MFMA32(BC[0], AC[1], ac[1][0]); SB(); C1; SB(); \
        ac[0][1] = MFMA32(BC[1], AC[0], ac[0][1]); SB(); BN[0] = ld_tr2(L, bF[0] + OFF, 4096); AN[0] = ld_b128(L, aF[0] + OFF); SB(); \
        ac[1][1] = MFMA32(BC[1], AC[1], ac[1][1]); SB(); AN[1] = ld_b128(L, aF[1] + OFF); BN[1] = ld_tr2(L, bF[1] + OFF, 4096); SB(); \
        ac[0][0] = MFMA32(BC[2], AC[2], ac[0][0]); SB(); BN[2] = ld_tr2(L, bF[0] + 2048 + OFF, 4096); AN[2] = ld_b128(L, aF[0] + 512 + OFF); SB(); \
        ac[1][0] = MFMA32(BC[2], AC[3], ac[1][0]); SB(); AN[3] = ld_b128(L, aF[1] + 512 + OFF); BN[3] = ld_tr2(L, bF[1] + 2048 + OFF, 4096); SB(); \
        ac[0][1] = MFMA32(BC[3], AC[2], ac[0][1]); \
        ac[1][1] = MFMA32(BC[3], AC[3], ac[1][1]); SB(); } while (0)
#pragma unroll 1
      for (unsigned n = 0; n < 7; ++n) {
        unsigned dA_, dB_;
        A_STEP(a0, b0, a1, b1, 8192, if (n == 6) vfab -= 16; PEEK(), { NEXT_PAIR(); dA_ = dA; dB_ = dB; });
        A_STEP(a1, b1, a0, b0, 0, { SIGNAL(); CHECK_LANDED(); }, { aF[0] += dA_; aF[1] += dA_; bF[0] += dB_; bF[1] += dB_; });
      }
    }
#define SEND(lq) do { _Pragma("unroll") for (int a = 0; a < 2; ++a) { \
        const f32x16 u = ac[(lq) & 1][(lq) >> 1]; \
        __builtin_amdgcn_raw_buffer_store_b128((v4u){pk_bf16(u[8 * a], u[8 * a + 1]), pk_bf16(u[8 * a + 2], u[8 * a + 3]), pk_bf16(u[8 * a + 4], u[8 * a + 5]), pk_bf16(u[8 * a + 6], u[8 * a + 7])}, wsr, \
            WS_PART + ((((m * 4 + cp) * 4 + (kap ^ (lq))) * 3 + ((lq) - 1)) * 8 + wave * 2 + a) * 1024 + lane * 16, 0, 16); } } while (0)
    f32x16 as = {};
    unsigned aS = A2OFF + sa + (wa * 2 + g1) * 2048 + (((qq + 4 * hh) ^ (g1 * 4)) * 32) + (lane & 3) * 8;
    unsigned bS[4];
#pragma unroll
    for (int u = 0; u < 4; ++u) bS[u] = sbb + (wb * 32 + (lane & 31)) * 128 + (((2 * u + hh) ^ ((lane >> 1) & 7)) << 4);
    v4u pt[3][2]; float gt;
    bf16x8 h0[4], c0[4], h1[4], c1[4];
#define B_FETCH(H, C, u, OFF) do { H[u] = ld_tr2(L, aS + (u) * 512 + OFF, 256); C[u] = ld_b128(L, bS[u] + OFF); } while (0)
#define B_STEP(HC, CC, HN, CN, OFF, FETCH, C0) do { \
      as = MFMA32(CC[0], HC[0], as); SB(); C0; SB(); if (FETCH) B_FETCH(HN, CN, 0, OFF); SB(); \
      as = MFMA32(CC[1], HC[1], as); SB(); if (FETCH) { B_FETCH(HN, CN, 1, OFF); B_FETCH(HN, CN, 2, OFF); } SB(); \
      as = MFMA32(CC[2], HC[2], as); SB(); if (FETCH) B_FETCH(HN, CN, 3, OFF); SB(); \
      as = MFMA32(CC[3], HC[3], as); SB(); } while (0)
#define B_PAIR(SIG, LAST, MID) do { \
      NEXT_PAIR(); \
      B_STEP(h0, c0, h1, c1, 8192, true, if (!(LAST)) PEEK()); \
      MID; \
      B_STEP(h1, c1, h0, c0, 0, !(LAST), { if (SIG) SIGNAL(); if (!(LAST)) { CHECK_LANDED(); aS += dA; bS[0] += dB; bS[1] += dB; bS[2] += dB; bS[3] += dB; } }); } while (0)
    SEND(1); SEND(2); SEND(3);
    SB();
    PEEK();
    const bool ready = ballot(pv == 0) == 0;
    if (!ready) { WAIT_VM(0); arrive(flagm, 3, 8 + cp, kap, 3u, tag, lane); W.ge(vfab, 1); }
    vfab += 16;
    gt = u2f(__builtin_amdgcn_raw_buffer_load_b32(wsr, WS_G + tok * 4, 0, 16));
#pragma unroll
    for (int u = 0; u < 4; ++u) { B_FETCH(h0, c0, u, 0); SB(); }
#pragma unroll 1
    for (unsigned n = 0; n < 4; ++n) {
      B_PAIR(true, false, (void)0);
      if (n == 0 && ready) { WAIT_VM(0); arrive(flagm, 3, 8 + cp, kap, 3u, tag, lane); }
    }
    v2f g2 = {gt, gt}, h2 = {1.f - gt, 1.f - gt};
    asm volatile("" : "+v"(g2), "+v"(h2));
#define UNPK(w) ((v2f){u2f((w) << 16), u2f((w) & 0xffff0000u)})
#pragma unroll
    for (int r = 0; r < 8; ++r) {
      const unsigned xw = xsk[r >> 1][r & 1], dw = dsk[r >> 1][r & 1];
      const v2f o = pfma(pmul(g2, UNPK(xw)), UNPK(dw), pmul(h2, (v2f){ac[0][0][2 * r], ac[0][0][2 * r + 1]}));
      ac[0][0][2 * r] = o.x; ac[0][0][2 * r + 1] = o.y;
    }
    asm volatile("" : "+v"(ac[0][0]));
    W.ge(L_R + 8, 1);
#pragma unroll
    for (int s = 0; s < 3; ++s)
#pragma unroll
      for (int a = 0; a < 2; ++a) pt[s][a] = __builtin_amdgcn_raw_buffer_load_b128(wsr, WS_PART + ((((m * 4 + cp) * 4 + kap) * 3 + s) * 8 + wave * 2 + a) * 1024 + lane * 16, 0, 16);
    SB();
#pragma unroll 1
    for (unsigned n = 0; n < 3; ++n) B_PAIR(false, false, (void)0);
    B_PAIR(false, true, (void)0);
    const __amdgpu_buffer_rsrc_t outr = __builtin_amdgcn_make_buffer_rsrc((void*)out, 0, 2048 * 512 * 4, 0x00020000);
#pragma unroll
    for (int a = 0; a < 4; ++a) {
      v4f o;
#pragma unroll
      for (int i = 0; i < 2; ++i) {
        const unsigned p0 = pt[0][a >> 1][(a & 1) * 2 + i], p1 = pt[1][a >> 1][(a & 1) * 2 + i], p2 = pt[2][a >> 1][(a & 1) * 2 + i];
        const v2f f = padd(padd(UNPK(p0), UNPK(p1)), UNPK(p2));
        const v2f r = pfma(g2, (v2f){as[4 * a + 2 * i], as[4 * a + 2 * i + 1]}, pfma(h2, f, (v2f){ac[0][0][4 * a + 2 * i], ac[0][0][4 * a + 2 * i + 1]}));
        o[2 * i] = r.x; o[2 * i + 1] = r.y;
      }
      *(LAS v4f*)(L + (L_DUMP + (wave < 3 ? wave * 4096 : -(int)L_DUMP) + (lane & 31) * 128 + (((2 * a + hh) ^ (lane & 7)) << 4))) = o;
    }
#pragma unroll
    for (int a = 0; a < 4; ++a) {
      const int r = a * 8 + (lane >> 3);
      const v4u o = *(const LAS v4u*)(L + (L_DUMP + (wave < 3 ? wave * 4096 : -(int)L_DUMP) + r * 128 + (((lane & 7) ^ (r & 7)) << 4)));
      const int tk = m * 128 + (kap & 1) * 64 + 16 * ((r & 15) >> 2) + (r & 3) + 8 * wa + 4 * (r >> 4);
      __builtin_amdgcn_raw_buffer_store_b128(o, outr, (tk * 512 + cp * 128 + (kap >> 1) * 64 + wb * 32) * 4 + (lane & 7) * 16, 0, 16);
    }
  }
}

extern "C" void kernel_launch(void* const* d_in, const int* in_sizes, int n_in, void* d_out, int out_size, void* d_ws, size_t ws_size, hipStream_t stream) {
  (void)in_sizes; (void)n_in; (void)out_size; (void)ws_size;
  (void)hipFuncSetAttribute((const void*)fused_kernel, hipFuncAttributeMaxDynamicSharedMemorySize, LDS_BYTES);
  fused_kernel<<<256, 768, LDS_BYTES, stream>>>((const u16*)d_in[0], (const u16*)d_in[1], (const u16*)d_in[2], (const u16*)d_in[3], (const u16*)d_in[4],
                                                (const u16*)d_in[5], (const u16*)d_in[6], (const u16*)d_in[7], (const u16*)d_in[8], (const u16*)d_in[10],
                                                (const u16*)d_in[11], (const u16*)d_in[12], (const u16*)d_in[13], (float*)d_out, (char*)d_ws);
}
```
